```python
import torch

def _softmax(x, axis=-1):
    return torch.softmax(x, dim=axis)
def _gelu(x):
    return torch.nn.functional.gelu(x, approximate="tanh")
def _silu(x):
    return torch.nn.functional.silu(x)
def _sigmoid(x):
    return torch.sigmoid(x)
def _layernorm(x, axis=-1, eps=1e-5):
    m = x.mean(dim=axis, keepdim=True)
    v = x.var(dim=axis, keepdim=True, unbiased=False)
    return (x - m) / torch.sqrt(v + eps)
def _scatter_add(tgt, idx, src, axis=0):
    return tgt.clone().index_add_(axis, idx, src)
def _segment_sum(src, seg, n):
    out = torch.zeros((n,) + src.shape[1:], dtype=src.dtype)
    return out.index_add_(0, seg, src)
def _pad(x, pw):
    flat = tuple(v for pair in reversed(pw) for v in pair)
    return torch.nn.functional.pad(x, flat)

kernel_name = 'gated_mix__gqa_block_cached__glu_gated_glu__decode'

def setup_inputs():

    _w = (lambda a: (lambda t: t.to(torch.bfloat16) if t.is_floating_point() else t)(torch.as_tensor(a)))
    def _A_setup():
        B, Sq, D = 4, 1, 512
        Hq, Hkv, Sk, Dh = 32, 8, 8192, 128
        assert Hq % Hkv == 0, (Hq, Hkv)
        return {
            "x": _w((lambda *s: torch.randn(*s, dtype=torch.bfloat16) * 0.05)(B, Sq, D)),
            "k_cache": _w((lambda *s: torch.randn(*s, dtype=torch.bfloat16) * 6.4)(B, Hkv, Sk, Dh)),
            "v_cache": _w((lambda *s: torch.randn(*s, dtype=torch.bfloat16) * 3.2)(B, Hkv, Sk, Dh)),
            "wq": _w((lambda *s: torch.randn(*s, dtype=torch.bfloat16) * 6.4)(D, Hq * Dh) * (D ** -0.5)),
            "wo": _w((lambda *s: torch.randn(*s, dtype=torch.bfloat16) * 3.2)(Hq * Dh, D) * ((Hq * Dh) ** -0.5)),
        }
    _A_in = _A_setup()

    def _B_setup():
        B, S, D, F = 4, 1, 512, 1792
        return {
            "x": _w((lambda *s: torch.randn(*s, dtype=torch.bfloat16) * 0.05)(B, S, D)),
            "w_gate": _w((lambda *s: torch.randn(*s, dtype=torch.bfloat16) * 3.2)(D, F)),
            "w_up": _w((lambda *s: torch.randn(*s, dtype=torch.bfloat16) * 0.2)(D, F)),
            "w_down": _w((lambda *s: torch.randn(*s, dtype=torch.bfloat16) * 0.2)(F, D)),
        }
    _B_in = _B_setup()

    return {
        "A_x": _A_in["x"],
        "A_k_cache": _A_in["k_cache"],
        "A_v_cache": _A_in["v_cache"],
        "A_wq": _A_in["wq"],
        "A_wo": _A_in["wo"],
        "B_x": _B_in["x"],
        "B_w_gate": _B_in["w_gate"],
        "B_w_up": _B_in["w_up"],
        "B_w_down": _B_in["w_down"],
        "gate_w": _w((lambda *s: torch.randn(*s, dtype=torch.bfloat16) * 0.05)(512) * 16.0),
    }

def reference(A_x, A_k_cache, A_v_cache, A_wq, A_wo, B_x, B_w_gate, B_w_up, B_w_down, gate_w):
    def _A_ref(x, k_cache, v_cache, wq, wo):
        B, Sq, D = x.shape
        Hq, Dh = 32, 128
        Hkv = k_cache.shape[1]
        rep = Hq // Hkv
        q = (lambda x, *d: x.permute(*d))((x @ wq).reshape(B, Sq, Hq, Dh), 0, 2, 1, 3)
        k = (lambda x, n, axis: torch.repeat_interleave(x, n, dim=axis))(k_cache, rep, axis=1)
        v = (lambda x, n, axis: torch.repeat_interleave(x, n, dim=axis))(v_cache, rep, axis=1)
        scores = torch.einsum("bhqd,bhkd->bhqk", q, k) * (Dh ** -0.5)
        p = _softmax(scores, axis=-1)
        o = torch.einsum("bhqk,bhkd->bhqd", p, v)
        return (lambda x, *d: x.permute(*d))(o, 0, 2, 1, 3).reshape(B, Sq, Hq * Dh) @ wo

    def _B_ref(x, w_gate, w_up, w_down):
        g = torch.einsum("bsd,df->bsf", x, w_gate)
        u = torch.einsum("bsd,df->bsf", x, w_up)
        h = _sigmoid(g) * u
        return torch.einsum("bsf,fd->bsd", h, w_down)

    x = A_x
    g = _sigmoid((x * gate_w).sum(-1, keepdims=True))
    a = _A_ref(x, A_k_cache, A_v_cache, A_wq, A_wo)
    b = _B_ref(x, B_w_gate, B_w_up, B_w_down)
    return g * a + (1 - g) * b
```

```cpp
#include <hip/hip_runtime.h>
#include <hip/hip_bf16.h>
#include <stdint.h>

namespace {

typedef unsigned int u32;
typedef unsigned short u16;
typedef unsigned long long u64;
typedef u32 u32x2 __attribute__((ext_vector_type(2)));
typedef u32 u32x4 __attribute__((ext_vector_type(4)));
typedef float f32x4 __attribute__((ext_vector_type(4)));
typedef short s16x4 __attribute__((ext_vector_type(4)));
typedef short s16x8 __attribute__((ext_vector_type(8)));
typedef __bf16 bf16x2 __attribute__((ext_vector_type(2)));

constexpr int NB = 4, DM = 512, HQ = 32, HKV = 8, SK = 8192, DH = 128, FF = 1792;
constexpr int NWG = 256, NTHR = 512, NWAVE = 8;
constexpr int TPW = 32;

#define WK 5
#define WV 8
#define QSTEP 18

constexpr u32 WS_EPOCH = 0;
constexpr u32 WS_RDY = 1024;
constexpr u32 WS_QG = 4096;
constexpr u32 WS_HG = WS_QG + NB * 4096 * 8;
constexpr u32 WS_AS = WS_HG + NB * FF * 8;
constexpr u32 WS_AO = WS_AS + NWG * 32;
constexpr u32 WS_BP = WS_AO + NWG * 2048;
constexpr u32 WS_END = WS_BP + 64 * 64 * 128;
constexpr u32 WS_DBG = 4u << 20;

constexpr u32 EMPTY = 0xFFFFFFFFu;
constexpr int AUX_SC1 = 16, AUX_NT = 2;
constexpr u32 SPIN_CAP = 1u << 17;

constexpr int L_PROJ = 0;
constexpr int L_GP = 256;
constexpr int L_MX = 272;
constexpr int L_LS = 304;
constexpr int L_QS = 336;
constexpr int L_RED = 592;
constexpr int L_RED2 = L_RED + 4096;
constexpr int L_TOTAL = L_RED2 + 4096;

__device__ __forceinline__ float as_f(u32 x) { return __builtin_bit_cast(float, x); }
__device__ __forceinline__ u32 as_u(float x) { return __builtin_bit_cast(u32, x); }
__device__ __forceinline__ u32 cvt_pk(float lo, float hi) {
  u32 r;
  asm("v_cvt_pk_bf16_f32 %0, %1, %2" : "=v"(r) : "v"(lo), "v"(hi));
  return r;
}
__device__ __forceinline__ float rbf(float x) { return as_f(cvt_pk(x, x) & 0xFFFF0000u); }
__device__ __forceinline__ float dot2(u32 a, u32 b, float c) {
  return __builtin_amdgcn_fdot2_f32_bf16(__builtin_bit_cast(bf16x2, a), __builtin_bit_cast(bf16x2, b), c, false);
}
template <int CTRL>
__device__ __forceinline__ float dpp(float x) {
  return as_f(__builtin_amdgcn_mov_dpp((int)as_u(x), CTRL, 0xf, 0xf, true));
}
constexpr int QP0 = 0x00, QP1 = 0x55, QP2 = 0xAA, QP3 = 0xFF, QX1 = 0xB1, QX2 = 0x4E;
constexpr int ROR1 = 0x121, ROR2 = 0x122, ROR4 = 0x124, ROR8 = 0x128;
__device__ __forceinline__ float xrow_max(float x) {
  auto s = __builtin_amdgcn_permlane16_swap(as_u(x), as_u(x), false, false);
  x = fmaxf(as_f(s[0]), as_f(s[1]));
  auto t = __builtin_amdgcn_permlane32_swap(as_u(x), as_u(x), false, false);
  return fmaxf(as_f(t[0]), as_f(t[1]));
}
__device__ __forceinline__ float xrow_sum(float x) {
  auto s = __builtin_amdgcn_permlane16_swap(as_u(x), as_u(x), false, false);
  x = as_f(s[0]) + as_f(s[1]);
  auto t = __builtin_amdgcn_permlane32_swap(as_u(x), as_u(x), false, false);
  return as_f(t[0]) + as_f(t[1]);
}
__device__ __forceinline__ float row_sum16(float v) {
  v += dpp<ROR8>(v);
  v += dpp<ROR4>(v);
  v += dpp<ROR2>(v);
  v += dpp<ROR1>(v);
  return v;
}
__device__ __forceinline__ f32x4 mfma32(u32x4 a, u32x4 b, f32x4 c) {
  return __builtin_amdgcn_mfma_f32_16x16x32_bf16(__builtin_bit_cast(s16x8, a), __builtin_bit_cast(s16x8, b), c, 0, 0, 0);
}
__device__ __forceinline__ f32x4 mfma16(u32x2 a, u32x2 b, f32x4 c) {
  return __builtin_amdgcn_mfma_f32_16x16x16bf16_1k(__builtin_bit_cast(s16x4, a), __builtin_bit_cast(s16x4, b), c, 0, 0, 0);
}
template <int N>
__device__ __forceinline__ void waitvm() {
  asm volatile("s_waitcnt vmcnt(%0)" ::"i"(N) : "memory");
}
__device__ __forceinline__ void wg_barrier() { asm volatile("s_waitcnt lgkmcnt(0)\n\ts_barrier" ::: "memory"); }
__device__ __forceinline__ void pin() { __builtin_amdgcn_sched_barrier(0); }
__device__ __forceinline__ u32 noempty(float v) { return min(as_u(v), 0xFFFFFFFEu); }
__device__ __forceinline__ float sigmoidf_(float x) { return 1.0f / (1.0f + __expf(-x)); }

typedef __amdgpu_buffer_rsrc_t rsrc_t;
__device__ __forceinline__ rsrc_t mk(const void* p, u32 bytes) {
  return __builtin_amdgcn_make_buffer_rsrc(const_cast<void*>(p), 0, bytes, 0x00020000);
}
#define LD128(r, v, s, aux) __builtin_amdgcn_raw_buffer_load_b128((r), (v), (s), (aux))
#define LD64(r, v, s, aux) __builtin_amdgcn_raw_buffer_load_b64((r), (v), (s), (aux))
#define ST128(x, r, v, aux) __builtin_amdgcn_raw_buffer_store_b128((x), (r), (v), 0, (aux))
#define ST64(x, r, v, aux) __builtin_amdgcn_raw_buffer_store_b64((x), (r), (v), 0, (aux))

#define STAMP(i)

__device__ __forceinline__ u32x2 granule_wait(rsrc_t rs, u32 off, u32x2 g, u32 TL, u32 TH) {
  bool ok = (g.y == TH) && ((g.x >> 16) == TL);
  if (__builtin_expect(!__all(ok), 0)) {
#pragma nounroll
    for (u32 spin = 0; spin < SPIN_CAP; ++spin) {
      __builtin_amdgcn_s_sleep(2);
      asm volatile("" : "+v"(off)::"memory");
      g = LD64(rs, off, 0, AUX_SC1);
      ok = (g.y == TH) && ((g.x >> 16) == TL);
      if (__all(ok)) break;
    }
  }
  return g;
}

__global__ void __launch_bounds__(NTHR, 2)
fused_decode(const u16* __restrict__ x, const u16* __restrict__ kc, const u16* __restrict__ vc, const u16* __restrict__ wq,
             const u16* __restrict__ wo, const u16* __restrict__ wg, const u16* __restrict__ wu, const u16* __restrict__ wd,
             const u16* __restrict__ gw, u16* out, unsigned char* ws) {
  __shared__ __attribute__((aligned(16))) float lds[L_TOTAL];
  const int tid = threadIdx.x, lane = tid & 63;
  const int w = __builtin_amdgcn_readfirstlane(tid >> 6);
  const int j = blockIdx.x, x8 = j & 7, i32 = j >> 3;
  const int g = lane >> 4, r16 = lane & 15;
  STAMP(0);

  const int chunk = x8 * 32 + i32;
  const int ab = chunk >> 6, akvh = (chunk >> 3) & 7;
  const int qcol0 = x8 * 512 + i32 * 16;
  const int ggrp = x8 + 8 * (i32 >> 3);
  const bool has_glu = ggrp < 28;
  const int f0 = (has_glu ? ggrp : x8) * 64 + (i32 & 7) * 8;
  const int rs_ = i32 >> 2, cs = i32 & 3, ia = x8 * 8 + rs_;

  const rsrc_t rws = mk(ws, WS_END);
  const rsrc_t rk = mk(kc, (u32)NB * HKV * SK * DH * 2), rv = mk(vc, (u32)NB * HKV * SK * DH * 2);

  const u32x2 ep = LD64(rws, WS_EPOCH, 0, AUX_SC1);
  {
    const u32x4 e4 = {EMPTY, EMPTY, EMPTY, EMPTY};
    if (w < 2) {
      ST128(e4, rws, WS_AO + chunk * 2048 + w * 1024 + lane * 16, AUX_SC1);
    } else if (w < 4) {
      const int idx = (w - 2) * 64 + lane, line = idx >> 3;
      ST128(e4, rws, WS_BP + (((line >> 2) * 16 + cs * 4 + (line & 3)) * 64 + ia) * 128 + (idx & 7) * 16, AUX_SC1);
    } else if (w == 4) {
      if (lane < 2) ST128(e4, rws, WS_AS + chunk * 32 + lane * 16, AUX_SC1);
    }
  }
  const int ck = w >> 1, rh = w & 1, row0 = rh * 256 + 4 * lane;
  u32x4 wr[4];
  u32x2 xb[4], gwv;
  {
    const u16* wbase = ck < 2 ? wq + qcol0 + ck * 8 : (ck == 2 ? wg : wu) + f0;
    const int wstride = ck < 2 ? 4096 : FF;
#pragma unroll
    for (int r = 0; r < 4; ++r) wr[r] = __builtin_nontemporal_load((const u32x4*)(wbase + (size_t)(row0 + r) * wstride));
#pragma unroll
    for (int b = 0; b < 4; ++b) xb[b] = *(const u32x2*)(x + b * DM + row0);
    gwv = *(const u32x2*)(gw + row0);
  }
  pin();
  const u32 tbase = (u32)chunk * 262144u + (u32)((w >> 2) * 128 + (w & 3)) * 1024u;
  const u32 koff = tbase + (u32)((r16 >> 2) * 256 + g * 64 + (r16 & 3) * 16);
  const u32 voff = tbase + (u32)lane * 16u;
  u32x4 kt[TPW];
#pragma unroll
  for (int i = 0; i < WK; ++i) kt[i] = LD128(rk, koff, i * 4096, AUX_NT);
  pin();

  {
    float acc[32];
#pragma unroll
    for (int cp = 0; cp < 4; ++cp) {
      const u32 lo01 = __builtin_amdgcn_perm(wr[1][cp], wr[0][cp], 0x05040100u), hi01 = __builtin_amdgcn_perm(wr[1][cp], wr[0][cp], 0x07060302u);
      const u32 lo23 = __builtin_amdgcn_perm(wr[3][cp], wr[2][cp], 0x05040100u), hi23 = __builtin_amdgcn_perm(wr[3][cp], wr[2][cp], 0x07060302u);
#pragma unroll
      for (int b = 0; b < 4; ++b) {
        acc[b * 8 + 2 * cp] = dot2(lo23, xb[b].y, dot2(lo01, xb[b].x, 0.f));
        acc[b * 8 + 2 * cp + 1] = dot2(hi23, xb[b].y, dot2(hi01, xb[b].x, 0.f));
      }
    }
#pragma unroll
    for (int i = 0; i < 16; ++i) {
      auto s = __builtin_amdgcn_permlane32_swap(as_u(acc[i]), as_u(acc[i + 16]), false, false);
      acc[i] = as_f(s[0]) + as_f(s[1]);
    }
#pragma unroll
    for (int i = 0; i < 8; ++i) {
      auto s = __builtin_amdgcn_permlane16_swap(as_u(acc[i]), as_u(acc[i + 8]), false, false);
      acc[i] = row_sum16(as_f(s[0]) + as_f(s[1]));
    }
    if (r16 == 0) {
      *(f32x4*)&lds[L_PROJ + w * 32 + g * 8] = f32x4{acc[0], acc[1], acc[2], acc[3]};
      *(f32x4*)&lds[L_PROJ + w * 32 + g * 8 + 4] = f32x4{acc[4], acc[5], acc[6], acc[7]};
    }
    if (ck == 1) {
      const float g0 = as_f(gwv.x << 16), g1 = as_f(gwv.x & 0xFFFF0000u), g2 = as_f(gwv.y << 16), g3 = as_f(gwv.y & 0xFFFF0000u);
      float gp[4];
#pragma unroll
      for (int b = 0; b < 4; ++b) {
        float t = rbf(as_f(xb[b].x << 16) * g0) + rbf(as_f(xb[b].x & 0xFFFF0000u) * g1);
        t += rbf(as_f(xb[b].y << 16) * g2) + rbf(as_f(xb[b].y & 0xFFFF0000u) * g3);
        gp[b] = xrow_sum(row_sum16(t));
      }
      if (lane == 0) *(f32x4*)&lds[L_GP + rh * 4] = f32x4{gp[0], gp[1], gp[2], gp[3]};
    }
  }
  const u64 e1 = (((u64)__builtin_amdgcn_readfirstlane(ep.y) << 32) | (u32)__builtin_amdgcn_readfirstlane(ep.x)) + 1ull;
  const u32 TL = (u32)e1 & 0xFFFFu, TH = (u32)(e1 >> 16);
  STAMP(1);
  waitvm<WK>();
  wg_barrier();
  if (w == 0) {
    const int c2 = r16 >> 3, col = r16 & 7;
    const float qv = lds[L_PROJ + (2 * c2) * 32 + g * 8 + col] + lds[L_PROJ + (2 * c2 + 1) * 32 + g * 8 + col];
    const u32x2 gr = {(TL << 16) | (cvt_pk(qv, qv) & 0xFFFFu), TH};
    ST64(gr, rws, WS_QG + (u32)(g * 4096 + qcol0 + r16) * 8u, AUX_SC1);
  } else if (w == 1) {
    if (lane < 32 && has_glu) {
      const float gt = rbf(lds[L_PROJ + 4 * 32 + lane] + lds[L_PROJ + 5 * 32 + lane]);
      const float up = rbf(lds[L_PROJ + 6 * 32 + lane] + lds[L_PROJ + 7 * 32 + lane]);
      const float hv = rbf(sigmoidf_(gt)) * up;
      const u32x2 gr = {(TL << 16) | (cvt_pk(hv, hv) & 0xFFFFu), TH};
      ST64(gr, rws, WS_HG + (u32)((lane >> 3) * FF + f0 + (lane & 7)) * 8u, AUX_SC1);
    }
  } else if (w == 2) {
    if (lane == 0) {
      const u32x2 gr = {TL << 16, TH};
      ST64(gr, rws, WS_RDY + (u32)j * 8u, AUX_SC1);
    }
  }
  pin();

  const u32 qgoff = WS_QG + (u32)(ab * 4096 + akvh * 512 + w * 64 + lane) * 8u;
  u32x2 qg;
#pragma unroll
  for (int i = WK; i < TPW; ++i) {
    waitvm<WK - 1>();
    kt[i] = LD128(rk, koff, i * 4096, AUX_NT);
    if (i == QSTEP) qg = LD64(rws, qgoff, 0, AUX_SC1);
    if (i == QSTEP + WK + 1) {
      qg = granule_wait(rws, qgoff, qg, TL, TH);
      ((u16*)&lds[L_QS])[w * 64 + lane] = (u16)qg.x;
    }
    pin();
  }
  STAMP(2);
  wg_barrier();
  const u32x4 qf = *(const u32x4*)((const u16*)&lds[L_QS] + (r16 >> 2) * 128 + g * 32 + (r16 & 3) * 8);
  STAMP(3);

  float s[TPW];
  u32x4 vt[WV];
  u32x4 wot[2], wdt;
  u32x2 hg, rdy;
  const u32 hgoff = WS_HG + (u32)((r16 >> 2) * FF + 28 * ia + 4 * (w < 7 ? w : 6) + g) * 8u;
  u32 rdyoff;
  {
    const int l5 = lane & 31, cA = ((l5 >> 3) * 8 + x8) * 8 + (l5 & 7);
    const int jA = (cA & 31) * 8 + (cA >> 5);
    const int jB = (lane & 7) + 8 * ((lane >> 3) * 4 + ((j & 15) >> 2));
    rdyoff = WS_RDY + (u32)(w == 3 ? jB : jA) * 8u;
  }
#pragma unroll
  for (int i = 0; i < TPW; ++i) {
    const f32x4 d = mfma32(kt[i], qf, f32x4{0.f, 0.f, 0.f, 0.f});
    s[i] = (dpp<QP0>(d[0]) + dpp<QP1>(d[1])) + (dpp<QP2>(d[2]) + dpp<QP3>(d[3]));
    if (i < WV) vt[i] = LD128(rv, voff, i * 4096, AUX_NT);
    if (i == WV) {
      const u16* wop = wo + (size_t)(x8 * 512 + rs_ * 64 + 8 * w + 2 * g) * DM + cs * 128 + r16 * 8;
      wot[0] = __builtin_nontemporal_load((const u32x4*)wop);
      wot[1] = __builtin_nontemporal_load((const u32x4*)(wop + DM));
      wdt = __builtin_nontemporal_load((const u32x4*)(wd + (size_t)(28 * ia + 4 * (w < 7 ? w : 6) + g) * DM + cs * 128 + r16 * 8));
      hg = LD64(rws, hgoff, 0, AUX_SC1);
      rdy = LD64(rws, rdyoff, 0, AUX_SC1);
    }
    pin();
  }
  STAMP(4);
  constexpr float C2 = 0.088388347648318440f * 1.4426950408889634f;
  float mw = s[0];
#pragma unroll
  for (int i = 1; i < TPW; ++i) mw = fmaxf(mw, s[i]);
  mw = xrow_max(mw) * C2;
  float lw = 0.f;
  u32 pk[TPW];
#pragma unroll
  for (int i = 0; i < TPW; ++i) {
    const float p = __builtin_amdgcn_exp2f(fmaf(s[i], C2, -mw));
    lw += p;
    pk[i] = cvt_pk(p, p);
  }
  lw = xrow_sum(lw);
  if (g == 0 && (r16 & 3) == 0) {
    lds[L_MX + w * 4 + (r16 >> 2)] = mw;
    lds[L_LS + w * 4 + (r16 >> 2)] = lw;
  }
  const int u_ = r16 & 3;
  const u32 M0 = u_ == 0 ? 0xFFFFu : (u_ == 1 ? 0xFFFF0000u : 0u), M1 = u_ == 2 ? 0xFFFFu : (u_ == 3 ? 0xFFFF0000u : 0u);
  pin();

  f32x4 olo = {0.f, 0.f, 0.f, 0.f}, ohi = {0.f, 0.f, 0.f, 0.f};
  f32x4 glo = {0.f, 0.f, 0.f, 0.f}, ghi = {0.f, 0.f, 0.f, 0.f};
  float gate = 0.f, fw = 0.f;
#pragma unroll
  for (int n = 0; n < TPW; ++n) {
    const u32x4 v = vt[n % WV];
    const u32x2 a = {pk[n] & M0, pk[n] & M1};
    olo = mfma16(a, u32x2{v[0], v[1]}, olo);
    ohi = mfma16(a, u32x2{v[2], v[3]}, ohi);
    if (n + WV < TPW) vt[n % WV] = LD128(rv, voff, (n + WV) * 4096, AUX_NT);
    if (n == WV + 2) {
      hg = granule_wait(rws, hgoff, hg, TL, TH);
      const u32 hb = w < 7 ? (hg.x & 0xFFFFu) * 0x10001u : 0u;
      const u32x2 ah = {hb & M0, hb & M1};
      glo = mfma16(ah, u32x2{wdt[0], wdt[1]}, glo);
      ghi = mfma16(ah, u32x2{wdt[2], wdt[3]}, ghi);
      gate = rbf(sigmoidf_(rbf(lds[L_GP + g] + lds[L_GP + 4 + g])));
      if (w == 2 || (w == 3 && j < 64)) rdy = granule_wait(rws, rdyoff, rdy, TL, TH);
    }
    if (n == TPW - WV) {
      wg_barrier();
      float M = lds[L_MX + g];
#pragma unroll
      for (int ww = 1; ww < NWAVE; ++ww) M = fmaxf(M, lds[L_MX + ww * 4 + g]);
      fw = __builtin_amdgcn_exp2f(lds[L_MX + w * 4 + g] - M);
    }
    pin();
  }
  STAMP(5);
  *(f32x4*)&lds[L_RED + w * 512 + lane * 8] = olo * fw;
  *(f32x4*)&lds[L_RED + w * 512 + lane * 8 + 4] = ohi * fw;
  wg_barrier();
  if (tid < 128) {
    f32x4 v = *(const f32x4*)&lds[L_RED + tid * 4];
#pragma unroll
    for (int ww = 1; ww < NWAVE; ++ww) v += *(const f32x4*)&lds[L_RED + ww * 512 + tid * 4];
    const u32x4 o = {noempty(v[0]), noempty(v[1]), noempty(v[2]), noempty(v[3])};
    ST128(o, rws, WS_AO + chunk * 2048 + tid * 16, AUX_SC1);
  } else if (tid < 132) {
    const int hd = tid - 128;
    float M = lds[L_MX + hd];
#pragma unroll
    for (int ww = 1; ww < NWAVE; ++ww) M = fmaxf(M, lds[L_MX + ww * 4 + hd]);
    float L = 0.f;
#pragma unroll
    for (int ww = 0; ww < NWAVE; ++ww) L += lds[L_LS + ww * 4 + hd] * __builtin_amdgcn_exp2f(lds[L_MX + ww * 4 + hd] - M);
    const u32x2 o = {noempty(M), noempty(L)};
    ST64(o, rws, WS_AS + chunk * 32 + hd * 8, AUX_SC1);
  }
  STAMP(6);
  {
    const int pb = r16 >> 2;
    const u32 c0 = (u32)((pb * 8 + x8) * 8 + 2 * u_);
    u32 oo = WS_AO + c0 * 2048u + (u32)(rs_ * 64 + 8 * w + 2 * g) * 4u;
    u32 so = WS_AS + c0 * 32u + (u32)(rs_ >> 1) * 8u;
    u32x2 o0, o1, s0, s1;
#pragma nounroll
    for (u32 spin = 0; spin < SPIN_CAP; ++spin) {
      asm volatile("" : "+v"(oo), "+v"(so)::"memory");
      o0 = LD64(rws, oo, 0, AUX_SC1);
      o1 = LD64(rws, oo + 2048u, 0, AUX_SC1);
      s0 = LD64(rws, so, 0, AUX_SC1);
      s1 = LD64(rws, so + 32u, 0, AUX_SC1);
      const u32 mx = max(max(max(o0.x, o0.y), max(o1.x, o1.y)), max(max(s0.x, s0.y), max(s1.x, s1.y)));
      if (__all(mx != EMPTY)) break;
      __builtin_amdgcn_s_sleep(1);
    }
    STAMP(7);
    float M = fmaxf(as_f(s0.x), as_f(s1.x));
    M = fmaxf(M, dpp<QX1>(M));
    M = fmaxf(M, dpp<QX2>(M));
    const float w0 = __builtin_amdgcn_exp2f(as_f(s0.x) - M), w1 = __builtin_amdgcn_exp2f(as_f(s1.x) - M);
    float L = w0 * as_f(s0.y) + w1 * as_f(s1.y);
    float e0 = w0 * as_f(o0.x) + w1 * as_f(o1.x), e1v = w0 * as_f(o0.y) + w1 * as_f(o1.y);
    L += dpp<QX1>(L);
    e0 += dpp<QX1>(e0);
    e1v += dpp<QX1>(e1v);
    L += dpp<QX2>(L);
    e0 += dpp<QX2>(e0);
    e1v += dpp<QX2>(e1v);
    const float inv = 1.0f / L;
    const u32 p0 = cvt_pk(e0 * inv, e0 * inv), p1 = cvt_pk(e1v * inv, e1v * inv);
    f32x4 alo = {0.f, 0.f, 0.f, 0.f}, ahi = {0.f, 0.f, 0.f, 0.f};
    alo = mfma16(u32x2{p0 & M0, p0 & M1}, u32x2{wot[0][0], wot[0][1]}, alo);
    ahi = mfma16(u32x2{p0 & M0, p0 & M1}, u32x2{wot[0][2], wot[0][3]}, ahi);
    alo = mfma16(u32x2{p1 & M0, p1 & M1}, u32x2{wot[1][0], wot[1][1]}, alo);
    ahi = mfma16(u32x2{p1 & M0, p1 & M1}, u32x2{wot[1][2], wot[1][3]}, ahi);
    const float og = rbf(1.0f - gate);
    *(f32x4*)&lds[L_RED2 + w * 512 + lane * 8] = alo * gate + glo * og;
    *(f32x4*)&lds[L_RED2 + w * 512 + lane * 8 + 4] = ahi * gate + ghi * og;
  }
  wg_barrier();
  if (tid < 128) {
    f32x4 v = *(const f32x4*)&lds[L_RED2 + tid * 4];
#pragma unroll
    for (int ww = 1; ww < NWAVE; ++ww) v += *(const f32x4*)&lds[L_RED2 + ww * 512 + tid * 4];
    const u32x4 o = {noempty(v[0]), noempty(v[1]), noempty(v[2]), noempty(v[3])};
    const int line = tid >> 3;
    ST128(o, rws, WS_BP + (((line >> 2) * 16 + cs * 4 + (line & 3)) * 64 + ia) * 128 + (tid & 7) * 16, AUX_SC1);
  }
  STAMP(8);
  if (w == 2 && j < 64) {
    u32 bo = WS_BP + (u32)(j * 64 + (lane >> 3)) * 128u + (u32)(lane & 7) * 16u;
    u32x4 pv[8];
#pragma nounroll
    for (u32 spin = 0; spin < SPIN_CAP; ++spin) {
      asm volatile("" : "+v"(bo)::"memory");
      u32 mx = 0;
#pragma unroll
      for (int i = 0; i < 8; ++i) pv[i] = LD128(rws, bo, i * 1024, AUX_SC1);
#pragma unroll
      for (int i = 0; i < 8; ++i) mx = max(max(mx, max(pv[i][0], pv[i][1])), max(pv[i][2], pv[i][3]));
      if (__all(mx != EMPTY)) break;
      __builtin_amdgcn_s_sleep(1);
    }
    f32x4 v = __builtin_bit_cast(f32x4, pv[0]);
#pragma unroll
    for (int i = 1; i < 8; ++i) v += __builtin_bit_cast(f32x4, pv[i]);
#pragma unroll
    for (int e = 0; e < 4; ++e) {
      float t = v[e];
      t += dpp<ROR8>(t);
      v[e] = xrow_sum(t);
    }
    if (lane < 8) {
      const u32x2 o = {cvt_pk(v[0], v[1]), cvt_pk(v[2], v[3])};
      *(u32x2*)(out + (j >> 4) * DM + (j & 15) * 32 + lane * 4) = o;
    }
    if (j == 0 && lane == 0) {
      const u32x2 ne = {(u32)e1, (u32)(e1 >> 32)};
      ST64(ne, rws, WS_EPOCH, AUX_SC1);
    }
    STAMP(9);
  }
}

}

extern "C" void kernel_launch(void* const* d_in, const int* in_sizes, int n_in, void* d_out, int out_size, void* d_ws,
                              size_t ws_size, hipStream_t stream) {
  (void)in_sizes; (void)n_in; (void)out_size; (void)ws_size;
  fused_decode<<<NWG, NTHR, 0, stream>>>((const u16*)d_in[0], (const u16*)d_in[1], (const u16*)d_in[2], (const u16*)d_in[3],
                                         (const u16*)d_in[4], (const u16*)d_in[6], (const u16*)d_in[7], (const u16*)d_in[8],
                                         (const u16*)d_in[9], (u16*)d_out, (unsigned char*)d_ws);
}
```

```cpp
#include <hip/hip_runtime.h>
#include <hip/hip_bf16.h>
#include <stdint.h>

namespace {

typedef unsigned int u32;
typedef unsigned short u16;
typedef unsigned long long u64;
typedef u32 u32x2 __attribute__((ext_vector_type(2)));
typedef u32 u32x4 __attribute__((ext_vector_type(4)));
typedef float f32x4 __attribute__((ext_vector_type(4)));
typedef short s16x4 __attribute__((ext_vector_type(4)));
typedef short s16x8 __attribute__((ext_vector_type(8)));
typedef __bf16 bf16x2 __attribute__((ext_vector_type(2)));

constexpr int NB = 4, DM = 512, HQ = 32, HKV = 8, SK = 8192, DH = 128, FF = 1792;
constexpr int NWG = 256, NTHR = 512, NWAVE = 8;
constexpr int TPW = 32;

#define WK 5
#define WV 8
#define WV1 5
#define POLL_SLEEP 2
#define PSTEP (WK + 2)
#define PLAG 2
#define SSTEP 16
#define QSTEP 27

static_assert(QSTEP < TPW && QSTEP + WK + 1 >= TPW && QSTEP + WK + 1 < TPW + WV1, "q check rides on the first V loads");
static_assert(PSTEP > WK && PSTEP + PLAG + WK <= SSTEP && SSTEP + WK + 1 <= QSTEP, "schedule");
static_assert(WV1 <= WV && WV - WV1 <= 14 && TPW - WV > 4, "schedule");

constexpr u32 WS_EPOCH = 0;
constexpr u32 WS_RDY = 1024;
constexpr u32 WS_QG = 4096;
constexpr u32 WS_HG = WS_QG + 4 * NB * 4096 * 8;
constexpr u32 WS_AS = WS_HG + 2 * 4 * NB * FF * 8;
constexpr u32 WS_AO = WS_AS + NWG * 32;
constexpr u32 WS_BP = WS_AO + NWG * 2048;
constexpr u32 WS_END = WS_BP + 64 * 64 * 128;
constexpr u32 WS_DBG = 4u << 20;

constexpr u32 EMPTY = 0xFFFFFFFFu;
constexpr u32 OOB = 0x7F000000u;
constexpr int AUX_SC1 = 16, AUX_NT = 2;
constexpr u32 SPIN_CAP = 1u << 17;

constexpr int L_GT = 0;
constexpr int L_SC = 8;
constexpr int L_CQ = 12;
constexpr int L_CS = 13;
constexpr int L_CP = 14;
constexpr int L_MX = 16;
constexpr int L_LS = 48;
constexpr int L_QS = 80;
constexpr int L_RED = 336;
constexpr int L_RED2 = L_RED + 4096;
constexpr int L_TOTAL = L_RED2 + 4096;

__device__ __forceinline__ float as_f(u32 x) { return __builtin_bit_cast(float, x); }
__device__ __forceinline__ u32 as_u(float x) { return __builtin_bit_cast(u32, x); }
__device__ __forceinline__ u32 cvt_pk(float lo, float hi) {
  u32 r;
  asm("v_cvt_pk_bf16_f32 %0, %1, %2" : "=v"(r) : "v"(lo), "v"(hi));
  return r;
}
__device__ __forceinline__ float rbf(float x) { return as_f(cvt_pk(x, x) & 0xFFFF0000u); }
__device__ __forceinline__ float dot2(u32 a, u32 b, float c) {
  return __builtin_amdgcn_fdot2_f32_bf16(__builtin_bit_cast(bf16x2, a), __builtin_bit_cast(bf16x2, b), c, false);
}
template <int CTRL>
__device__ __forceinline__ float dpp(float x) {
  return as_f(__builtin_amdgcn_mov_dpp((int)as_u(x), CTRL, 0xf, 0xf, true));
}
constexpr int QP0 = 0x00, QP1 = 0x55, QP2 = 0xAA, QP3 = 0xFF, QX1 = 0xB1, QX2 = 0x4E;
constexpr int ROR1 = 0x121, ROR2 = 0x122, ROR4 = 0x124, ROR8 = 0x128;
__device__ __forceinline__ float xrow_max(float x) {
  auto s = __builtin_amdgcn_permlane16_swap(as_u(x), as_u(x), false, false);
  x = fmaxf(as_f(s[0]), as_f(s[1]));
  auto t = __builtin_amdgcn_permlane32_swap(as_u(x), as_u(x), false, false);
  return fmaxf(as_f(t[0]), as_f(t[1]));
}
__device__ __forceinline__ float xrow_sum(float x) {
  auto s = __builtin_amdgcn_permlane16_swap(as_u(x), as_u(x), false, false);
  x = as_f(s[0]) + as_f(s[1]);
  auto t = __builtin_amdgcn_permlane32_swap(as_u(x), as_u(x), false, false);
  return as_f(t[0]) + as_f(t[1]);
}
__device__ __forceinline__ float row_sum16(float v) {
  v += dpp<ROR8>(v);
  v += dpp<ROR4>(v);
  v += dpp<ROR2>(v);
  v += dpp<ROR1>(v);
  return v;
}
__device__ __forceinline__ f32x4 mfma32(u32x4 a, u32x4 b, f32x4 c) {
  return __builtin_amdgcn_mfma_f32_16x16x32_bf16(__builtin_bit_cast(s16x8, a), __builtin_bit_cast(s16x8, b), c, 0, 0, 0);
}
__device__ __forceinline__ f32x4 mfma16(u32x2 a, u32x2 b, f32x4 c) {
  return __builtin_amdgcn_mfma_f32_16x16x16bf16_1k(__builtin_bit_cast(s16x4, a), __builtin_bit_cast(s16x4, b), c, 0, 0, 0);
}
template <int N>
__device__ __forceinline__ void waitvm() {
  asm volatile("s_waitcnt vmcnt(%0)" ::"i"(N) : "memory");
}
__device__ __forceinline__ void wg_barrier() { asm volatile("s_waitcnt lgkmcnt(0)\n\ts_barrier" ::: "memory"); }
__device__ __forceinline__ void pin() { __builtin_amdgcn_sched_barrier(0); }
__device__ __forceinline__ u32 noempty(float v) { return min(as_u(v), 0xFFFFFFFEu); }
__device__ __forceinline__ float sigmoidf_(float x) { return 1.0f / (1.0f + __expf(-x)); }

__device__ __forceinline__ u32 lds_arrive(float* c) {
  asm volatile("s_waitcnt lgkmcnt(0)" ::: "memory");
  return __hip_atomic_fetch_add((u32*)c, 1u, __ATOMIC_RELAXED, __HIP_MEMORY_SCOPE_WORKGROUP);
}
__device__ __forceinline__ void lds_await(float* c, u32 n) {
  while (__hip_atomic_load((u32*)c, __ATOMIC_RELAXED, __HIP_MEMORY_SCOPE_WORKGROUP) < n) __builtin_amdgcn_s_sleep(1);
  asm volatile("" ::: "memory");
}
template <int V>
struct ic {
  static constexpr int value = V;
};
template <int I, int N, class F>
__device__ __forceinline__ void static_for(F&& f) {
  if constexpr (I < N) {
    f(ic<I>{});
    static_for<I + 1, N>(f);
  }
}

typedef __amdgpu_buffer_rsrc_t rsrc_t;
__device__ __forceinline__ rsrc_t mk(const void* p, u32 bytes) {
  return __builtin_amdgcn_make_buffer_rsrc(const_cast<void*>(p), 0, bytes, 0x00020000);
}
#define LD128(r, v, s, aux) __builtin_amdgcn_raw_buffer_load_b128((r), (v), (s), (aux))
#define LD64(r, v, s, aux) __builtin_amdgcn_raw_buffer_load_b64((r), (v), (s), (aux))
#define ST128(x, r, v, aux) __builtin_amdgcn_raw_buffer_store_b128((x), (r), (v), 0, (aux))
#define ST64(x, r, v, aux) __builtin_amdgcn_raw_buffer_store_b64((x), (r), (v), 0, (aux))

#define STAMP(i)
#define STAMP_FLUSH()

__device__ __forceinline__ u32x2 mk_granule(float v, u32 TL, u32 TH) {
  const u32 u = as_u(v);
  return u32x2{((u + 0x7Fu + ((u >> 8) & 1u)) >> 8) | TL, TH};
}
__device__ __forceinline__ bool g_ok(u32x2 g, u32 TL, u32 TH) { return ((g.x ^ TL) < 0x1000000u) & (g.y == TH); }
__device__ __forceinline__ float g_val(u32x2 g) { return as_f(g.x << 8); }
#define TR(c)
template <int N, u32 STRIDE>
__device__ __forceinline__ void granule_wait(rsrc_t rs, u32 off, u32x2 (&g)[N], u32 TL, u32 TH) {
  bool ok = true;
#pragma unroll
  for (int k = 0; k < N; ++k) ok &= g_ok(g[k], TL, TH);
  if (__builtin_expect(!__all(ok), 0)) {
#pragma nounroll
    for (u32 spin = 0; spin < SPIN_CAP; ++spin) {
      __builtin_amdgcn_s_sleep(4);
      asm volatile("" : "+v"(off)::"memory");
      ok = true;
#pragma unroll
      for (int k = 0; k < N; ++k) g[k] = LD64(rs, off, k * STRIDE, AUX_SC1);
#pragma unroll
      for (int k = 0; k < N; ++k) ok &= g_ok(g[k], TL, TH);
      if (__all(ok)) break;
    }
  }
}
__device__ __forceinline__ void proj_tile(u32x4 ra, u32x4 rb, const u32 (&xp)[4], float (&o)[8]) {
  float acc[32];
#pragma unroll
  for (int cp = 0; cp < 4; ++cp) {
    const u32 lo = __builtin_amdgcn_perm(rb[cp], ra[cp], 0x05040100u), hi = __builtin_amdgcn_perm(rb[cp], ra[cp], 0x07060302u);
#pragma unroll
    for (int b = 0; b < 4; ++b) {
      acc[b * 8 + 2 * cp] = dot2(lo, xp[b], 0.f);
      acc[b * 8 + 2 * cp + 1] = dot2(hi, xp[b], 0.f);
    }
  }
#pragma unroll
  for (int i = 0; i < 16; ++i) {
    auto t = __builtin_amdgcn_permlane32_swap(as_u(acc[i]), as_u(acc[i + 16]), false, false);
    acc[i] = as_f(t[0]) + as_f(t[1]);
  }
#pragma unroll
  for (int i = 0; i < 8; ++i) {
    auto t = __builtin_amdgcn_permlane16_swap(as_u(acc[i]), as_u(acc[i + 8]), false, false);
    const float v = as_f(t[0]) + as_f(t[1]);
    o[i] = v + dpp<ROR8>(v);
  }
}

__global__ void __launch_bounds__(NTHR, 2)
fused_decode(const u16* __restrict__ x, const u16* __restrict__ kc, const u16* __restrict__ vc, const u16* __restrict__ wq,
             const u16* __restrict__ wo, const u16* __restrict__ wg, const u16* __restrict__ wu, const u16* __restrict__ wd,
             const u16* __restrict__ gw, u16* out, unsigned char* ws) {
  __shared__ __attribute__((aligned(16))) float lds[L_TOTAL];
  const int tid = threadIdx.x, lane = tid & 63;
  const int w = __builtin_amdgcn_readfirstlane(tid >> 6);
  const int j = blockIdx.x, x8 = j & 7, i32 = j >> 3;
  const int g = lane >> 4, r16 = lane & 15;
  STAMP(0);
  if (tid < 3) ((u32*)lds)[L_CQ + tid] = 0u;

  const int chunk = x8 * 32 + i32;
  const int ab = chunk >> 6, akvh = (chunk >> 3) & 7;
  const int kq = j & 3, qcb = j >> 2;
  const bool has_glu = j < 224;
  const int gmat = j >= 112 ? 1 : 0, gcb = (j - 112 * gmat) >> 2;
  const int rs_ = i32 >> 2, cs = i32 & 3, ia = x8 * 8 + rs_;

  const rsrc_t rws = mk(ws, WS_END);
  const rsrc_t rk = mk(kc, (u32)NB * HKV * SK * DH * 2), rv = mk(vc, (u32)NB * HKV * SK * DH * 2);

  const int prow = kq * 128 + 16 * w + 2 * (lane >> 3);
  u32x4 wa, wb, ga, gb;
  u32 xl;
  {
    const u16* p = wq + (size_t)prow * 4096 + qcb * 64 + (lane & 7) * 8;
    wa = __builtin_nontemporal_load((const u32x4*)p);
    wb = __builtin_nontemporal_load((const u32x4*)(p + 4096));
    const u16* pg = (gmat ? wu : wg) + (size_t)prow * FF + (has_glu ? gcb : 0) * 64 + (lane & 7) * 8;
    ga = __builtin_nontemporal_load((const u32x4*)pg);
    gb = __builtin_nontemporal_load((const u32x4*)(pg + FF));
    xl = *(const u32*)(x + ((lane >> 3) & 3) * DM + kq * 128 + 16 * w + 2 * (lane & 7));
  }
  pin();
  const u32 tbase = (u32)chunk * 262144u + (u32)((w >> 2) * 128 + (w & 3)) * 1024u;
  const u32 koff = tbase + (u32)((r16 >> 2) * 256 + g * 64 + (r16 & 3) * 16);
  const u32 voff = tbase + (u32)lane * 16u;
  u32x4 kt[TPW];
#pragma unroll
  for (int i = 0; i < WK; ++i) kt[i] = LD128(rk, koff, i * 4096, AUX_NT);
  pin();
  const u64 e1 = *(const __attribute__((address_space(4))) u64*)(uintptr_t)(ws + WS_EPOCH) + 1ull;
  const u32 TL = (u32)e1 << 24, TH = (u32)(e1 >> 8);
  wg_barrier();
  {
    const u32x4 e4 = {EMPTY, EMPTY, EMPTY, EMPTY};
    const int line = (w & 1) * 8 + (lane >> 3);
    const u32 oa = WS_AO + chunk * 2048 + (w & 1) * 1024 + lane * 16;
    const u32 ob = WS_BP + (((line >> 2) * 16 + cs * 4 + (line & 3)) * 64 + ia) * 128 + (lane & 7) * 16;
    const u32 os = (w == 4 && lane < 2) ? WS_AS + chunk * 32 + lane * 16 : OOB;
    ST128(e4, rws, w < 2 ? oa : (w < 4 ? ob : os), AUX_SC1);
  }
  pin();

  const int u_ = r16 & 3;
  const u32 M0 = u_ == 0 ? 0xFFFFu : (u_ == 1 ? 0xFFFF0000u : 0u), M1 = u_ == 2 ? 0xFFFFu : (u_ == 3 ? 0xFFFF0000u : 0u);
  u32 qgoff = WS_QG + (u32)(((lane >> 5) * 4 + ab) * 4096 + akvh * 512 + w * 64 + 2 * (lane & 31)) * 8u;
  const u32 hgoff = WS_HG + (u32)((u_ * 4 + (r16 >> 2)) * FF + 28 * ia + 4 * (w < 7 ? w : 6) + g) * 8u;
  u32 rdyoff;
  {
    const int l5 = lane & 31, cA = ((l5 >> 3) * 8 + x8) * 8 + (l5 & 7);
    const int cS = (chunk & ~7) + (lane & 7);
    const int cx = lane < 32 ? cA : cS;
    const int jA = (cx & 31) * 8 + (cx >> 5);
    const int jB = (lane & 7) + 8 * ((lane >> 3) * 4 + ((j & 15) >> 2));
    rdyoff = (w == 2 || (w == 3 && j < 64)) ? WS_RDY + (u32)(w == 3 ? jB : jA) * 8u : OOB;
  }
  u32x4 qg[2];
  u32x4 wot[2], wdt;
  u32x2 hg[2], rdy[1], xg, gwv;
  static_for<WK, TPW>([&](auto ii) {
    constexpr int i = ii.value;
    if constexpr (i == PSTEP) {
      u32 xp[4];
#pragma unroll
      for (int b = 0; b < 4; ++b) xp[b] = __builtin_amdgcn_ds_bpermute((b * 8 + (lane >> 3)) * 4, xl);
      float o[8];
      proj_tile(wa, wb, xp, o);
      float* dst = &lds[L_RED2 + w * 256 + g * 64 + (r16 & 7) * 8];
      if (r16 < 8) {
        *(f32x4*)dst = f32x4{o[0], o[1], o[2], o[3]};
        *(f32x4*)(dst + 4) = f32x4{o[4], o[5], o[6], o[7]};
      }
      proj_tile(ga, gb, xp, o);
      if (r16 < 8) {
        *(f32x4*)(dst + 2048) = f32x4{o[0], o[1], o[2], o[3]};
        *(f32x4*)(dst + 2052) = f32x4{o[4], o[5], o[6], o[7]};
      }
      STAMP(1);
      waitvm<PSTEP - WK>();
      if (lane == 0) lds_arrive(&lds[L_CP]);
      pin();
    }
    if constexpr (i == PSTEP + PLAG) {
      lds_await(&lds[L_CP], NWAVE);
      const float* src = &lds[L_RED2 + (w >> 2) * 2048 + (w & 3) * 64 + lane];
      float v = src[0];
#pragma unroll
      for (int ww = 1; ww < NWAVE; ++ww) v += src[ww * 256];
      const u32 off = w < 4 ? WS_QG + (u32)((kq * 4 + w) * 4096 + qcb * 64 + lane) * 8u
                            : (has_glu ? WS_HG + (u32)(((gmat * 4 + kq) * 4 + (w - 4)) * FF + gcb * 64 + lane) * 8u : OOB);
      ST64(mk_granule(v, TL, TH), rws, off, AUX_SC1);
      if (tid == 0) ST64((u32x2{TL, TH}), rws, WS_RDY + (u32)j * 8u, AUX_SC1);
      pin();
    }
    waitvm<WK - 1 + (i < PSTEP ? 1 : 0) + ((i >= PSTEP + PLAG && i < PSTEP + PLAG + WK) ? 1 : 0) +
           ((i > SSTEP && i <= SSTEP + WK) ? 5 : 0) + (i > QSTEP ? 3 : 0)>();
    TR(i - WK);
    kt[i] = LD128(rk, koff, i * 4096, AUX_NT);
    if constexpr (i == SSTEP) {
      const u16* wop = wo + (size_t)(x8 * 512 + rs_ * 64 + 8 * w + 2 * g) * DM + cs * 128 + r16 * 8;
      wot[0] = __builtin_nontemporal_load((const u32x4*)wop);
      wot[1] = __builtin_nontemporal_load((const u32x4*)(wop + DM));
      wdt = __builtin_nontemporal_load((const u32x4*)(wd + (size_t)(28 * ia + 4 * (w < 7 ? w : 6) + g) * DM + cs * 128 + r16 * 8));
      xg = *(const u32x2*)(x + w * 256 + lane * 4);
      gwv = *(const u32x2*)(gw + (w & 1) * 256 + lane * 4);
    }
    if constexpr (i == SSTEP + WK + 1) {
      float t = rbf(as_f(xg.x << 16) * as_f(gwv.x << 16)) + rbf(as_f(xg.x & 0xFFFF0000u) * as_f(gwv.x & 0xFFFF0000u));
      t += rbf(as_f(xg.y << 16) * as_f(gwv.y << 16)) + rbf(as_f(xg.y & 0xFFFF0000u) * as_f(gwv.y & 0xFFFF0000u));
      t = xrow_sum(row_sum16(t));
      if (lane == 0) lds[L_GT + w] = t;
    }
    if constexpr (i == QSTEP) {
      qg[0] = LD128(rws, qgoff, 0, AUX_SC1);
      qg[1] = LD128(rws, qgoff, 2 * NB * 4096 * 8, AUX_SC1);
      rdy[0] = LD64(rws, rdyoff, 0, AUX_SC1);
    }
    pin();
  });
  u32x4 vt[TPW];
  static_for<0, WV1>([&](auto nn) {
    constexpr int n = nn.value;
    waitvm<WK - 1 + (n + TPW <= QSTEP + WK ? 3 : 0)>();
    if constexpr (n < WK) TR(TPW + n - WK);
    vt[n] = LD128(rv, voff, n * 4096, AUX_NT);
    if constexpr (n + TPW == QSTEP + WK + 1) {
      auto q_ok = [&]() {
        bool ok = true;
#pragma unroll
        for (int k = 0; k < 2; ++k) ok &= g_ok(u32x2{qg[k][0], qg[k][1]}, TL, TH) & g_ok(u32x2{qg[k][2], qg[k][3]}, TL, TH);
        return ok;
      };
      if (__builtin_expect(!__all(q_ok()), 0)) {
#pragma nounroll
        for (u32 spin = 0; spin < SPIN_CAP; ++spin) {
          __builtin_amdgcn_s_sleep(4);
          asm volatile("" : "+v"(qgoff)::"memory");
          qg[0] = LD128(rws, qgoff, 0, AUX_SC1);
          qg[1] = LD128(rws, qgoff, 2 * NB * 4096 * 8, AUX_SC1);
          if (__all(q_ok())) break;
        }
      }
      const float h0 = as_f(qg[0][0] << 8) + as_f(qg[1][0] << 8), h1 = as_f(qg[0][2] << 8) + as_f(qg[1][2] << 8);
      auto t0 = __builtin_amdgcn_permlane32_swap(as_u(h0), as_u(h0), false, false);
      auto t1 = __builtin_amdgcn_permlane32_swap(as_u(h1), as_u(h1), false, false);
      if (lane < 32) ((u32*)lds)[L_QS + w * 32 + lane] = cvt_pk(as_f(t0[0]) + as_f(t0[1]), as_f(t1[0]) + as_f(t1[1]));
      if (lane == 0) lds_arrive(&lds[L_CQ]);
    }
    pin();
  });
  STAMP(2);
  lds_await(&lds[L_CQ], NWAVE);
  const u32x4 qf = *(const u32x4*)((const u16*)&lds[L_QS] + (r16 >> 2) * 128 + g * 32 + (r16 & 3) * 8);
  STAMP(3);

  float s[TPW];
  static_for<0, TPW / 4>([&](auto ii) {
    constexpr int i = ii.value * 4;
    f32x4 d[4];
#pragma unroll
    for (int t = 0; t < 4; ++t) d[t] = mfma32(kt[i + t], qf, f32x4{0.f, 0.f, 0.f, 0.f});
#pragma unroll
    for (int t = 0; t < 4; ++t) s[i + t] = (dpp<QP0>(d[t][0]) + dpp<QP1>(d[t][1])) + (dpp<QP2>(d[t][2]) + dpp<QP3>(d[t][3]));
    if constexpr (i >= 4) {
#pragma unroll
      for (int t = 0; t < 2; ++t)
        if ((i - 4) / 2 + t + WV1 < WV) vt[(i - 4) / 2 + t + WV1] = LD128(rv, voff, ((i - 4) / 2 + t + WV1) * 4096, AUX_NT);
    }
    pin();
  });
  STAMP(4);
  constexpr float C2 = 0.088388347648318440f * 1.4426950408889634f;
  float mw = s[0];
#pragma unroll
  for (int i = 1; i < TPW; ++i) mw = fmaxf(mw, s[i]);
  mw = xrow_max(mw) * C2;
  float lw = 0.f;
  u32 pk[TPW];
#pragma unroll
  for (int i = 0; i < TPW; ++i) {
    const float p = __builtin_amdgcn_exp2f(fmaf(s[i], C2, -mw));
    lw += p;
    pk[i] = cvt_pk(p, p);
  }
  lw = xrow_sum(lw);
  if (g == 0 && u_ == 0) {
    lds[L_MX + w * 4 + (r16 >> 2)] = mw;
    lds[L_LS + w * 4 + (r16 >> 2)] = lw;
  }
  {
    u32 old = 0;
    if (lane == 0) old = lds_arrive(&lds[L_CS]);
    const bool last = __builtin_amdgcn_readfirstlane(old) == NWAVE - 1;
    float M = 0.f, L = 0.f;
    if (last) {
      asm volatile("" ::: "memory");
      const int hd = lane & 3;
      M = lds[L_MX + hd];
#pragma unroll
      for (int ww = 1; ww < NWAVE; ++ww) M = fmaxf(M, lds[L_MX + ww * 4 + hd]);
#pragma unroll
      for (int ww = 0; ww < NWAVE; ++ww) L += lds[L_LS + ww * 4 + hd] * __builtin_amdgcn_exp2f(lds[L_MX + ww * 4 + hd] - M);
    }
    ST64((u32x2{noempty(M), noempty(L)}), rws, (last && lane < 4) ? WS_AS + chunk * 32 + lane * 8 : OOB, AUX_SC1);
  }
  pin();

  f32x4 olo = {0.f, 0.f, 0.f, 0.f}, ohi = {0.f, 0.f, 0.f, 0.f};
  float fw = 0.f;
  u32x2 st;
  u32 stoff = w == 2 ? WS_AS + (u32)(chunk & ~7) * 32u + (u32)(lane & 31) * 8u : OOB;
  static_for<0, TPW>([&](auto nn) {
    constexpr int n = nn.value;
    const u32x4 v = vt[n];
    const u32x2 a = {pk[n] & M0, pk[n] & M1};
    olo = mfma16(a, u32x2{v[0], v[1]}, olo);
    ohi = mfma16(a, u32x2{v[2], v[3]}, ohi);
    if constexpr (n + WV < TPW) vt[n + WV] = LD128(rv, voff, (n + WV) * 4096, AUX_NT);
    if constexpr (n == TPW - WV - 4) {
      if (w == 2 || (w == 3 && j < 64)) granule_wait<1, 0>(rws, rdyoff, rdy, TL, TH);
    }
    if constexpr (n == TPW - WV - 3) {
      st = LD64(rws, stoff, 0, AUX_SC1);
      hg[0] = LD64(rws, hgoff, 0, AUX_SC1);
      hg[1] = LD64(rws, hgoff, 4 * NB * FF * 8, AUX_SC1);
    }
    if constexpr (n == TPW - WV) {
      lds_await(&lds[L_CS], NWAVE);
      float M = lds[L_MX + g];
#pragma unroll
      for (int ww = 1; ww < NWAVE; ++ww) M = fmaxf(M, lds[L_MX + ww * 4 + g]);
      fw = __builtin_amdgcn_exp2f(lds[L_MX + w * 4 + g] - M);
    }
    if constexpr (n == TPW - 3) {
      if (w == 2) {
        if (__builtin_expect(!__all(max(st.x, st.y) != EMPTY), 0)) {
#pragma nounroll
          for (u32 spin = 0; spin < SPIN_CAP; ++spin) {
            __builtin_amdgcn_s_sleep(2);
            asm volatile("" : "+v"(stoff)::"memory");
            st = LD64(rws, stoff, 0, AUX_SC1);
            if (__all(max(st.x, st.y) != EMPTY)) break;
          }
        }
        float Mwg = lds[L_MX + (lane & 3)];
#pragma unroll
        for (int ww = 1; ww < NWAVE; ++ww) Mwg = fmaxf(Mwg, lds[L_MX + ww * 4 + (lane & 3)]);
        float M = as_f(st.x);
        M = fmaxf(M, dpp<ROR4>(M));
        M = fmaxf(M, dpp<ROR8>(M));
        M = xrow_max(M);
        float L = as_f(st.y) * __builtin_amdgcn_exp2f(as_f(st.x) - M);
        L += dpp<ROR4>(L);
        L += dpp<ROR8>(L);
        L = xrow_sum(L) * 0.5f;
        if (lane < 4) lds[L_SC + lane] = __builtin_amdgcn_exp2f(Mwg - M) / L;
      }
    }
    pin();
  });
  STAMP(5);
  *(f32x4*)&lds[L_RED + w * 512 + lane * 8] = olo * fw;
  *(f32x4*)&lds[L_RED + w * 512 + lane * 8 + 4] = ohi * fw;
  wg_barrier();
  if (tid < 128) {
    f32x4 v = *(const f32x4*)&lds[L_RED + tid * 4];
#pragma unroll
    for (int ww = 1; ww < NWAVE; ++ww) v += *(const f32x4*)&lds[L_RED + ww * 512 + tid * 4];
    v *= lds[L_SC + (tid >> 5)];
    const u32x4 o = {noempty(v[0]), noempty(v[1]), noempty(v[2]), noempty(v[3])};
    ST128(o, rws, WS_AO + chunk * 2048 + tid * 16, AUX_SC1);
  }
  STAMP(6);
  {
    f32x4 glo = {0.f, 0.f, 0.f, 0.f}, ghi = {0.f, 0.f, 0.f, 0.f};
    {
      granule_wait<2, 4 * NB * FF * 8>(rws, hgoff, hg, TL, TH);
      float gt = g_val(hg[0]), up = g_val(hg[1]);
      gt += dpp<QX1>(gt);
      up += dpp<QX1>(up);
      gt += dpp<QX2>(gt);
      up += dpp<QX2>(up);
      const float hv = rbf(sigmoidf_(rbf(gt))) * rbf(up);
      const u32 hb = w < 7 ? cvt_pk(hv, hv) : 0u;
      const u32x2 ah = {hb & M0, hb & M1};
      glo = mfma16(ah, u32x2{wdt[0], wdt[1]}, glo);
      ghi = mfma16(ah, u32x2{wdt[2], wdt[3]}, ghi);
    }
    const float gate = rbf(sigmoidf_(rbf(lds[L_GT + 2 * g] + lds[L_GT + 2 * g + 1])));
    const float og = rbf(1.0f - gate);
    u32 oo = WS_AO + (u32)(((r16 >> 2) * 8 + x8) * 8 + 2 * u_) * 2048u + (u32)(rs_ * 64 + 8 * w + 2 * g) * 4u;
    u32x2 o0 = {EMPTY, EMPTY}, o1 = {EMPTY, EMPTY};
    bool need = true;
#pragma nounroll
    for (u32 spin = 0; spin < SPIN_CAP; ++spin) {
      asm volatile("" : "+v"(oo)::"memory");
      if (need) {
        o0 = LD64(rws, oo, 0, AUX_SC1);
        o1 = LD64(rws, oo + 2048u, 0, AUX_SC1);
        need = max(max(o0.x, o0.y), max(o1.x, o1.y)) == EMPTY;
      }
      if (!__any(need)) break;
      __builtin_amdgcn_s_sleep(POLL_SLEEP);
    }
    STAMP(7);
    float e0 = as_f(o0.x) + as_f(o1.x), e1v = as_f(o0.y) + as_f(o1.y);
    e0 += dpp<QX1>(e0);
    e1v += dpp<QX1>(e1v);
    e0 += dpp<QX2>(e0);
    e1v += dpp<QX2>(e1v);
    const u32 p0 = cvt_pk(e0, e0), p1 = cvt_pk(e1v, e1v);
    f32x4 alo = {0.f, 0.f, 0.f, 0.f}, ahi = {0.f, 0.f, 0.f, 0.f};
    alo = mfma16(u32x2{p0 & M0, p0 & M1}, u32x2{wot[0][0], wot[0][1]}, alo);
    ahi = mfma16(u32x2{p0 & M0, p0 & M1}, u32x2{wot[0][2], wot[0][3]}, ahi);
    alo = mfma16(u32x2{p1 & M0, p1 & M1}, u32x2{wot[1][0], wot[1][1]}, alo);
    ahi = mfma16(u32x2{p1 & M0, p1 & M1}, u32x2{wot[1][2], wot[1][3]}, ahi);
    *(f32x4*)&lds[L_RED2 + w * 512 + lane * 8] = alo * gate + glo * og;
    *(f32x4*)&lds[L_RED2 + w * 512 + lane * 8 + 4] = ahi * gate + ghi * og;
  }
  wg_barrier();
  if (tid < 128) {
    f32x4 v = *(const f32x4*)&lds[L_RED2 + tid * 4];
#pragma unroll
    for (int ww = 1; ww < NWAVE; ++ww) v += *(const f32x4*)&lds[L_RED2 + ww * 512 + tid * 4];
    const u32x4 o = {noempty(v[0]), noempty(v[1]), noempty(v[2]), noempty(v[3])};
    const int line = tid >> 3;
    ST128(o, rws, WS_BP + (((line >> 2) * 16 + cs * 4 + (line & 3)) * 64 + ia) * 128 + (tid & 7) * 16, AUX_SC1);
  }
  STAMP(8);
  if (j < 64) {
    u32 bo = WS_BP + (u32)(j * 64 + w * 8 + (lane >> 3)) * 128u + (u32)(lane & 7) * 16u;
    u32x4 pv = {EMPTY, EMPTY, EMPTY, EMPTY};
    bool need = true;
#pragma nounroll
    for (u32 spin = 0; spin < SPIN_CAP; ++spin) {
      asm volatile("" : "+v"(bo)::"memory");
      if (need) {
        pv = LD128(rws, bo, 0, AUX_SC1);
        need = max(max(pv[0], pv[1]), max(pv[2], pv[3])) == EMPTY;
      }
      if (!__any(need)) break;
      __builtin_amdgcn_s_sleep(1);
    }
    f32x4 v = __builtin_bit_cast(f32x4, pv);
#pragma unroll
    for (int e = 0; e < 4; ++e) {
      float t = v[e];
      t += dpp<ROR8>(t);
      v[e] = xrow_sum(t);
    }
    if (lane < 8) *(f32x4*)&lds[L_RED + w * 32 + lane * 4] = v;
    wg_barrier();
    if (tid < 16) {
      float a0 = lds[L_RED + 2 * tid], a1 = lds[L_RED + 2 * tid + 1];
#pragma unroll
      for (int ww = 1; ww < NWAVE; ++ww) {
        a0 += lds[L_RED + ww * 32 + 2 * tid];
        a1 += lds[L_RED + ww * 32 + 2 * tid + 1];
      }
      *(u32*)(out + (j >> 4) * DM + (j & 15) * 32 + 2 * tid) = cvt_pk(a0, a1);
      if (j == 0 && tid == 0) {
        const u32x2 ne = {(u32)e1, (u32)(e1 >> 32)};
        ST64(ne, rws, WS_EPOCH, AUX_SC1);
      }
    }
    STAMP(9);
  }
  STAMP_FLUSH();
}

}

extern "C" void kernel_launch(void* const* d_in, const int* in_sizes, int n_in, void* d_out, int out_size, void* d_ws,
                              size_t ws_size, hipStream_t stream) {
  (void)in_sizes; (void)n_in; (void)out_size; (void)ws_size;
  fused_decode<<<NWG, NTHR, 0, stream>>>((const u16*)d_in[0], (const u16*)d_in[1], (const u16*)d_in[2], (const u16*)d_in[3],
                                         (const u16*)d_in[4], (const u16*)d_in[6], (const u16*)d_in[7], (const u16*)d_in[8],
                                         (const u16*)d_in[9], (u16*)d_out, (unsigned char*)d_ws);
}
```

```cpp
#include <hip/hip_runtime.h>
#include <hip/hip_bf16.h>
#include <stdint.h>

namespace {

typedef unsigned int u32;
typedef unsigned short u16;
typedef unsigned long long u64;
typedef u32 u32x2 __attribute__((ext_vector_type(2)));
typedef u32 u32x4 __attribute__((ext_vector_type(4)));
typedef float f32x4 __attribute__((ext_vector_type(4)));
typedef short s16x4 __attribute__((ext_vector_type(4)));
typedef short s16x8 __attribute__((ext_vector_type(8)));
typedef __bf16 bf16x2 __attribute__((ext_vector_type(2)));

constexpr int NB = 4, DM = 512, HQ = 32, HKV = 8, SK = 8192, DH = 128, FF = 1792;
constexpr int NWG = 256, NTHR = 512, NWAVE = 8;
constexpr int TPW = 32;

#define TOUCH
#define POLL_SLEEP 0
#define WK 5
#define WV 8
#define WV1 5
#define PSTEP (WK + 2)
#define PLAG 2
#define SSTEP 16
#define QSTEP 27

static_assert(QSTEP < TPW && QSTEP + WK + 1 >= TPW && QSTEP + WK + 1 < TPW + WV1, "q check rides on the first V loads");
static_assert(PSTEP > WK && PSTEP + PLAG + WK <= SSTEP && SSTEP + WK + 1 <= QSTEP, "schedule");
static_assert(WV1 <= WV && WV - WV1 <= 14 && TPW - WV > 4, "schedule");

constexpr u32 WS_EPOCH = 0;
constexpr u32 WS_RDY = 1024;
constexpr u32 WS_QG = 4096;
constexpr u32 WS_HG = WS_QG + 4 * NB * 4096 * 8;
constexpr u32 WS_AS = WS_HG + 2 * 4 * NB * FF * 8;
constexpr u32 WS_AO = WS_AS + NWG * 32;
constexpr u32 WS_BP = WS_AO + NWG * 2048;
constexpr u32 WS_END = WS_BP + 64 * 64 * 128;
constexpr u32 WS_DBG = 4u << 20;

constexpr u32 EMPTY = 0xFFFFFFFFu;
constexpr u32 OOB = 0x7F000000u;
constexpr int AUX_SC1 = 16, AUX_NT = 2;
constexpr u32 SPIN_CAP = 1u << 17;

constexpr int L_GT = 0;
constexpr int L_SC = 8;
constexpr int L_CQ = 12;
constexpr int L_CS = 13;
constexpr int L_CP = 14;
constexpr int L_MX = 16;
constexpr int L_LS = 48;
constexpr int L_QS = 80;
constexpr int L_RED = 336;
constexpr int L_RED2 = L_RED + 4096;
constexpr int L_TOTAL = L_RED2 + 4096;

__device__ __forceinline__ float as_f(u32 x) { return __builtin_bit_cast(float, x); }
__device__ __forceinline__ u32 as_u(float x) { return __builtin_bit_cast(u32, x); }
__device__ __forceinline__ u32 cvt_pk(float lo, float hi) {
  u32 r;
  asm("v_cvt_pk_bf16_f32 %0, %1, %2" : "=v"(r) : "v"(lo), "v"(hi));
  return r;
}
__device__ __forceinline__ float rbf(float x) { return as_f(cvt_pk(x, x) & 0xFFFF0000u); }
__device__ __forceinline__ float dot2(u32 a, u32 b, float c) {
  return __builtin_amdgcn_fdot2_f32_bf16(__builtin_bit_cast(bf16x2, a), __builtin_bit_cast(bf16x2, b), c, false);
}
template <int CTRL>
__device__ __forceinline__ float dpp(float x) {
  return as_f(__builtin_amdgcn_mov_dpp((int)as_u(x), CTRL, 0xf, 0xf, true));
}
constexpr int QP0 = 0x00, QP1 = 0x55, QP2 = 0xAA, QP3 = 0xFF, QX1 = 0xB1, QX2 = 0x4E;
constexpr int ROR1 = 0x121, ROR2 = 0x122, ROR4 = 0x124, ROR8 = 0x128;
__device__ __forceinline__ float xrow_max(float x) {
  auto s = __builtin_amdgcn_permlane16_swap(as_u(x), as_u(x), false, false);
  x = fmaxf(as_f(s[0]), as_f(s[1]));
  auto t = __builtin_amdgcn_permlane32_swap(as_u(x), as_u(x), false, false);
  return fmaxf(as_f(t[0]), as_f(t[1]));
}
__device__ __forceinline__ float xrow_sum(float x) {
  auto s = __builtin_amdgcn_permlane16_swap(as_u(x), as_u(x), false, false);
  x = as_f(s[0]) + as_f(s[1]);
  auto t = __builtin_amdgcn_permlane32_swap(as_u(x), as_u(x), false, false);
  return as_f(t[0]) + as_f(t[1]);
}
__device__ __forceinline__ float row_sum16(float v) {
  v += dpp<ROR8>(v);
  v += dpp<ROR4>(v);
  v += dpp<ROR2>(v);
  v += dpp<ROR1>(v);
  return v;
}
__device__ __forceinline__ f32x4 mfma32(u32x4 a, u32x4 b, f32x4 c) {
  return __builtin_amdgcn_mfma_f32_16x16x32_bf16(__builtin_bit_cast(s16x8, a), __builtin_bit_cast(s16x8, b), c, 0, 0, 0);
}
__device__ __forceinline__ f32x4 mfma16(u32x2 a, u32x2 b, f32x4 c) {
  return __builtin_amdgcn_mfma_f32_16x16x16bf16_1k(__builtin_bit_cast(s16x4, a), __builtin_bit_cast(s16x4, b), c, 0, 0, 0);
}
template <int N>
__device__ __forceinline__ void waitvm() {
  asm volatile("s_waitcnt vmcnt(%0)" ::"i"(N) : "memory");
}
__device__ __forceinline__ void wg_barrier() { asm volatile("s_waitcnt lgkmcnt(0)\n\ts_barrier" ::: "memory"); }
__device__ __forceinline__ void pin() { __builtin_amdgcn_sched_barrier(0); }
__device__ __forceinline__ u32 noempty(float v) { return min(as_u(v), 0xFFFFFFFEu); }
__device__ __forceinline__ float sigmoidf_(float x) { return 1.0f / (1.0f + __expf(-x)); }

__device__ __forceinline__ u32 lds_arrive(float* c) {
  asm volatile("s_waitcnt lgkmcnt(0)" ::: "memory");
  return __hip_atomic_fetch_add((u32*)c, 1u, __ATOMIC_RELAXED, __HIP_MEMORY_SCOPE_WORKGROUP);
}
__device__ __forceinline__ void lds_await(float* c, u32 n) {
  while (__hip_atomic_load((u32*)c, __ATOMIC_RELAXED, __HIP_MEMORY_SCOPE_WORKGROUP) < n) __builtin_amdgcn_s_sleep(1);
  asm volatile("" ::: "memory");
}
template <int V>
struct ic {
  static constexpr int value = V;
};
template <int I, int N, class F>
__device__ __forceinline__ void static_for(F&& f) {
  if constexpr (I < N) {
    f(ic<I>{});
    static_for<I + 1, N>(f);
  }
}

typedef __amdgpu_buffer_rsrc_t rsrc_t;
__device__ __forceinline__ rsrc_t mk(const void* p, u32 bytes) {
  return __builtin_amdgcn_make_buffer_rsrc(const_cast<void*>(p), 0, bytes, 0x00020000);
}
#define LD128(r, v, s, aux) __builtin_amdgcn_raw_buffer_load_b128((r), (v), (s), (aux))
#define LD64(r, v, s, aux) __builtin_amdgcn_raw_buffer_load_b64((r), (v), (s), (aux))
#define ST128(x, r, v, aux) __builtin_amdgcn_raw_buffer_store_b128((x), (r), (v), 0, (aux))
#define ST64(x, r, v, aux) __builtin_amdgcn_raw_buffer_store_b64((x), (r), (v), 0, (aux))

#define STAMP(i)
#define STAMP_FLUSH()

__device__ __forceinline__ u32x2 mk_granule(float v, u32 TL, u32 TH) {
  const u32 u = as_u(v);
  return u32x2{((u + 0x7Fu + ((u >> 8) & 1u)) >> 8) | TL, TH};
}
__device__ __forceinline__ bool g_ok(u32x2 g, u32 TL, u32 TH) { return ((g.x ^ TL) < 0x1000000u) & (g.y == TH); }
__device__ __forceinline__ float g_val(u32x2 g) { return as_f(g.x << 8); }
#define TR(c)
template <int N, u32 STRIDE>
__device__ __forceinline__ void granule_wait(rsrc_t rs, u32 off, u32x2 (&g)[N], u32 TL, u32 TH) {
  bool ok = true;
#pragma unroll
  for (int k = 0; k < N; ++k) ok &= g_ok(g[k], TL, TH);
  if (__builtin_expect(!__all(ok), 0)) {
#pragma nounroll
    for (u32 spin = 0; spin < SPIN_CAP; ++spin) {
      __builtin_amdgcn_s_sleep(4);
      asm volatile("" : "+v"(off)::"memory");
      ok = true;
#pragma unroll
      for (int k = 0; k < N; ++k) g[k] = LD64(rs, off, k * STRIDE, AUX_SC1);
#pragma unroll
      for (int k = 0; k < N; ++k) ok &= g_ok(g[k], TL, TH);
      if (__all(ok)) break;
    }
  }
}
__device__ __forceinline__ void proj_tile(u32x4 ra, u32x4 rb, const u32 (&xp)[4], float (&o)[8]) {
  float acc[32];
#pragma unroll
  for (int cp = 0; cp < 4; ++cp) {
    const u32 lo = __builtin_amdgcn_perm(rb[cp], ra[cp], 0x05040100u), hi = __builtin_amdgcn_perm(rb[cp], ra[cp], 0x07060302u);
#pragma unroll
    for (int b = 0; b < 4; ++b) {
      acc[b * 8 + 2 * cp] = dot2(lo, xp[b], 0.f);
      acc[b * 8 + 2 * cp + 1] = dot2(hi, xp[b], 0.f);
    }
  }
#pragma unroll
  for (int i = 0; i < 16; ++i) {
    auto t = __builtin_amdgcn_permlane32_swap(as_u(acc[i]), as_u(acc[i + 16]), false, false);
    acc[i] = as_f(t[0]) + as_f(t[1]);
  }
#pragma unroll
  for (int i = 0; i < 8; ++i) {
    auto t = __builtin_amdgcn_permlane16_swap(as_u(acc[i]), as_u(acc[i + 8]), false, false);
    const float v = as_f(t[0]) + as_f(t[1]);
    o[i] = v + dpp<ROR8>(v);
  }
}

__global__ void __launch_bounds__(NTHR, 2)
fused_decode(const u16* __restrict__ x, const u16* __restrict__ kc, const u16* __restrict__ vc, const u16* __restrict__ wq,
             const u16* __restrict__ wo, const u16* __restrict__ wg, const u16* __restrict__ wu, const u16* __restrict__ wd,
             const u16* __restrict__ gw, u16* out, unsigned char* ws) {
  __shared__ __attribute__((aligned(16))) float lds[L_TOTAL];
  const int tid = threadIdx.x, lane = tid & 63;
  const int w = __builtin_amdgcn_readfirstlane(tid >> 6);
  const int j = blockIdx.x, x8 = j & 7, i32 = j >> 3;
  const int g = lane >> 4, r16 = lane & 15;
  STAMP(0);
  if (tid < 3) ((u32*)lds)[L_CQ + tid] = 0u;

  const int chunk = x8 * 32 + i32;
  const int ab = chunk >> 6, akvh = (chunk >> 3) & 7;
  const int kq = j & 3, qcb = j >> 2;
  const bool has_glu = j < 224;
  const int gmat = j >= 112 ? 1 : 0, gcb = (j - 112 * gmat) >> 2;
  const int rs_ = i32 >> 2, cs = i32 & 3, ia = x8 * 8 + rs_;

  const rsrc_t rws = mk(ws, WS_END);
  const rsrc_t rk = mk(kc, (u32)NB * HKV * SK * DH * 2), rv = mk(vc, (u32)NB * HKV * SK * DH * 2);

  const int prow = kq * 128 + 16 * w + 2 * (lane >> 3);
  u32x4 wa, wb, ga, gb;
  u32 xl;
  {
    const u16* p = wq + (size_t)prow * 4096 + qcb * 64 + (lane & 7) * 8;
    wa = __builtin_nontemporal_load((const u32x4*)p);
    wb = __builtin_nontemporal_load((const u32x4*)(p + 4096));
    const u16* pg = (gmat ? wu : wg) + (size_t)prow * FF + (has_glu ? gcb : 0) * 64 + (lane & 7) * 8;
    ga = __builtin_nontemporal_load((const u32x4*)pg);
    gb = __builtin_nontemporal_load((const u32x4*)(pg + FF));
    xl = *(const u32*)(x + ((lane >> 3) & 3) * DM + kq * 128 + 16 * w + 2 * (lane & 7));
  }
  pin();
  const u32 tbase = (u32)chunk * 262144u + (u32)((w >> 2) * 128 + (w & 3)) * 1024u;
  const u32 koff = tbase + (u32)((r16 >> 2) * 256 + g * 64 + (r16 & 3) * 16);
  const u32 voff = tbase + (u32)lane * 16u;
  u32x4 kt[TPW];
#pragma unroll
  for (int i = 0; i < WK; ++i) kt[i] = LD128(rk, koff, i * 4096, AUX_NT);
  pin();
  const u64 e1 = *(const __attribute__((address_space(4))) u64*)(uintptr_t)(ws + WS_EPOCH) + 1ull;
  const u32 TL = (u32)e1 << 24, TH = (u32)(e1 >> 8);
  wg_barrier();
  {
    const u32x4 e4 = {EMPTY, EMPTY, EMPTY, EMPTY};
    const int line = (w & 1) * 8 + (lane >> 3);
    const u32 oa = WS_AO + chunk * 2048 + (w & 1) * 1024 + lane * 16;
    const u32 ob = WS_BP + (((line >> 2) * 16 + cs * 4 + (line & 3)) * 64 + ia) * 128 + (lane & 7) * 16;
    const u32 os = (w == 4 && lane < 2) ? WS_AS + chunk * 32 + lane * 16 : OOB;
    ST128(e4, rws, w < 2 ? oa : (w < 4 ? ob : os), AUX_SC1);
  }
  pin();

  const int u_ = r16 & 3;
  const u32 M0 = u_ == 0 ? 0xFFFFu : (u_ == 1 ? 0xFFFF0000u : 0u), M1 = u_ == 2 ? 0xFFFFu : (u_ == 3 ? 0xFFFF0000u : 0u);
  u32 qgoff = WS_QG + (u32)(((lane >> 5) * 4 + ab) * 4096 + akvh * 512 + w * 64 + 2 * (lane & 31)) * 8u;
  const u32 hgoff = WS_HG + (u32)((u_ * 4 + (r16 >> 2)) * FF + 28 * ia + 4 * (w < 7 ? w : 6) + g) * 8u;
  u32 rdyoff;
  {
    const int l5 = lane & 31, cA = ((l5 >> 3) * 8 + x8) * 8 + (l5 & 7);
    const int cS = (chunk & ~7) + (lane & 7);
    const int cx = lane < 32 ? cA : cS;
    const int jA = (cx & 31) * 8 + (cx >> 5);
    const int jB = (lane & 7) + 8 * ((lane >> 3) * 4 + ((j & 15) >> 2));
    rdyoff = (w == 2 || (w == 3 && j < 64)) ? WS_RDY + (u32)(w == 3 ? jB : jA) * 8u : OOB;
  }
  u32x4 qg[2];
  u32x4 wot[2], wdt;
  u32x2 hg[2], rdy[1], xg, gwv;
  u32 touch = 0;
  static_for<WK, TPW>([&](auto ii) {
    constexpr int i = ii.value;
    if constexpr (i == PSTEP) {
      u32 xp[4];
#pragma unroll
      for (int b = 0; b < 4; ++b) xp[b] = __builtin_amdgcn_ds_bpermute((b * 8 + (lane >> 3)) * 4, xl);
      float o[8];
      proj_tile(wa, wb, xp, o);
      float* dst = &lds[L_RED2 + w * 256 + g * 64 + (r16 & 7) * 8];
      if (r16 < 8) {
        *(f32x4*)dst = f32x4{o[0], o[1], o[2], o[3]};
        *(f32x4*)(dst + 4) = f32x4{o[4], o[5], o[6], o[7]};
      }
      proj_tile(ga, gb, xp, o);
      if (r16 < 8) {
        *(f32x4*)(dst + 2048) = f32x4{o[0], o[1], o[2], o[3]};
        *(f32x4*)(dst + 2052) = f32x4{o[4], o[5], o[6], o[7]};
      }
      STAMP(1);
      waitvm<PSTEP - WK>();
      if (lane == 0) lds_arrive(&lds[L_CP]);
      pin();
    }
    if constexpr (i == PSTEP + PLAG) {
      lds_await(&lds[L_CP], NWAVE);
      const float* src = &lds[L_RED2 + (w >> 2) * 2048 + (w & 3) * 64 + lane];
      float v = src[0];
#pragma unroll
      for (int ww = 1; ww < NWAVE; ++ww) v += src[ww * 256];
      const u32 off = w < 4 ? WS_QG + (u32)((kq * 4 + w) * 4096 + qcb * 64 + lane) * 8u
                            : (has_glu ? WS_HG + (u32)(((gmat * 4 + kq) * 4 + (w - 4)) * FF + gcb * 64 + lane) * 8u : OOB);
      ST64(mk_granule(v, TL, TH), rws, off, AUX_SC1);
      if (tid == 0) ST64((u32x2{TL, TH}), rws, WS_RDY + (u32)j * 8u, AUX_SC1);
      pin();
    }
    waitvm<WK - 1 + (i < PSTEP ? 1 : 0) + ((i >= PSTEP + PLAG && i < PSTEP + PLAG + WK) ? 1 : 0) +
           ((i > SSTEP && i <= SSTEP + WK) ? 5 : 0) + (i > QSTEP ? 3 : 0)>();
    TR(i - WK);
    kt[i] = LD128(rk, koff, i * 4096, AUX_NT);
    if constexpr (i == SSTEP) {
      const u16* wop = wo + (size_t)(x8 * 512 + rs_ * 64 + 8 * w + 2 * g) * DM + cs * 128 + r16 * 8;
      wot[0] = __builtin_nontemporal_load((const u32x4*)wop);
      wot[1] = __builtin_nontemporal_load((const u32x4*)(wop + DM));
      wdt = __builtin_nontemporal_load((const u32x4*)(wd + (size_t)(28 * ia + 4 * (w < 7 ? w : 6) + g) * DM + cs * 128 + r16 * 8));
      touch = *(const volatile u32*)(out + (j & 63) * 32);
      xg = *(const u32x2*)(x + w * 256 + lane * 4);
      gwv = *(const u32x2*)(gw + (w & 1) * 256 + lane * 4);
    }
    if constexpr (i == SSTEP + WK + 1) {
      float t = rbf(as_f(xg.x << 16) * as_f(gwv.x << 16)) + rbf(as_f(xg.x & 0xFFFF0000u) * as_f(gwv.x & 0xFFFF0000u));
      t += rbf(as_f(xg.y << 16) * as_f(gwv.y << 16)) + rbf(as_f(xg.y & 0xFFFF0000u) * as_f(gwv.y & 0xFFFF0000u));
      t = xrow_sum(row_sum16(t));
      if (lane == 0) lds[L_GT + w] = t;
      asm volatile("" ::"v"(touch));
    }
    if constexpr (i == QSTEP) {
      qg[0] = LD128(rws, qgoff, 0, AUX_SC1);
      qg[1] = LD128(rws, qgoff, 2 * NB * 4096 * 8, AUX_SC1);
      rdy[0] = LD64(rws, rdyoff, 0, AUX_SC1);
    }
    pin();
  });
  u32x4 vt[TPW];
  static_for<0, WV1>([&](auto nn) {
    constexpr int n = nn.value;
    waitvm<WK - 1 + (n + TPW <= QSTEP + WK ? 3 : 0)>();
    if constexpr (n < WK) TR(TPW + n - WK);
    vt[n] = LD128(rv, voff, n * 4096, AUX_NT);
    if constexpr (n + TPW == QSTEP + WK + 1) {
      auto q_ok = [&]() {
        bool ok = true;
#pragma unroll
        for (int k = 0; k < 2; ++k) ok &= g_ok(u32x2{qg[k][0], qg[k][1]}, TL, TH) & g_ok(u32x2{qg[k][2], qg[k][3]}, TL, TH);
        return ok;
      };
      if (__builtin_expect(!__all(q_ok()), 0)) {
#pragma nounroll
        for (u32 spin = 0; spin < SPIN_CAP; ++spin) {
          __builtin_amdgcn_s_sleep(4);
          asm volatile("" : "+v"(qgoff)::"memory");
          qg[0] = LD128(rws, qgoff, 0, AUX_SC1);
          qg[1] = LD128(rws, qgoff, 2 * NB * 4096 * 8, AUX_SC1);
          if (__all(q_ok())) break;
        }
      }
      const float h0 = as_f(qg[0][0] << 8) + as_f(qg[1][0] << 8), h1 = as_f(qg[0][2] << 8) + as_f(qg[1][2] << 8);
      auto t0 = __builtin_amdgcn_permlane32_swap(as_u(h0), as_u(h0), false, false);
      auto t1 = __builtin_amdgcn_permlane32_swap(as_u(h1), as_u(h1), false, false);
      if (lane < 32) ((u32*)lds)[L_QS + w * 32 + lane] = cvt_pk(as_f(t0[0]) + as_f(t0[1]), as_f(t1[0]) + as_f(t1[1]));
      if (lane == 0) lds_arrive(&lds[L_CQ]);
    }
    pin();
  });
  STAMP(2);
  lds_await(&lds[L_CQ], NWAVE);
  const u32x4 qf = *(const u32x4*)((const u16*)&lds[L_QS] + (r16 >> 2) * 128 + g * 32 + (r16 & 3) * 8);
  STAMP(3);

  float s[TPW];
  static_for<0, TPW / 4>([&](auto ii) {
    constexpr int i = ii.value * 4;
    f32x4 d[4];
#pragma unroll
    for (int t = 0; t < 4; ++t) d[t] = mfma32(kt[i + t], qf, f32x4{0.f, 0.f, 0.f, 0.f});
#pragma unroll
    for (int t = 0; t < 4; ++t) s[i + t] = (dpp<QP0>(d[t][0]) + dpp<QP1>(d[t][1])) + (dpp<QP2>(d[t][2]) + dpp<QP3>(d[t][3]));
    if constexpr (i >= 4) {
#pragma unroll
      for (int t = 0; t < 2; ++t)
        if ((i - 4) / 2 + t + WV1 < WV) vt[(i - 4) / 2 + t + WV1] = LD128(rv, voff, ((i - 4) / 2 + t + WV1) * 4096, AUX_NT);
    }
    pin();
  });
  STAMP(4);
  constexpr float C2 = 0.088388347648318440f * 1.4426950408889634f;
  float mw = s[0];
#pragma unroll
  for (int i = 1; i < TPW; ++i) mw = fmaxf(mw, s[i]);
  mw = xrow_max(mw) * C2;
  float lw = 0.f;
  u32 pk[TPW];
#pragma unroll
  for (int i = 0; i < TPW; ++i) {
    const float p = __builtin_amdgcn_exp2f(fmaf(s[i], C2, -mw));
    lw += p;
    pk[i] = cvt_pk(p, p);
  }
  lw = xrow_sum(lw);
  if (g == 0 && u_ == 0) {
    lds[L_MX + w * 4 + (r16 >> 2)] = mw;
    lds[L_LS + w * 4 + (r16 >> 2)] = lw;
  }
  {
    u32 old = 0;
    if (lane == 0) old = lds_arrive(&lds[L_CS]);
    const bool last = __builtin_amdgcn_readfirstlane(old) == NWAVE - 1;
    float M = 0.f, L = 0.f;
    if (last) {
      asm volatile("" ::: "memory");
      const int hd = lane & 3;
      M = lds[L_MX + hd];
#pragma unroll
      for (int ww = 1; ww < NWAVE; ++ww) M = fmaxf(M, lds[L_MX + ww * 4 + hd]);
#pragma unroll
      for (int ww = 0; ww < NWAVE; ++ww) L += lds[L_LS + ww * 4 + hd] * __builtin_amdgcn_exp2f(lds[L_MX + ww * 4 + hd] - M);
    }
    ST64((u32x2{noempty(M), noempty(L)}), rws, (last && lane < 4) ? WS_AS + chunk * 32 + lane * 8 : OOB, AUX_SC1);
  }
  pin();

  f32x4 olo = {0.f, 0.f, 0.f, 0.f}, ohi = {0.f, 0.f, 0.f, 0.f};
  float fw = 0.f;
  u32x2 st;
  u32 stoff = w == 2 ? WS_AS + (u32)(chunk & ~7) * 32u + (u32)(lane & 31) * 8u : OOB;
  static_for<0, TPW>([&](auto nn) {
    constexpr int n = nn.value;
    const u32x4 v = vt[n];
    const u32x2 a = {pk[n] & M0, pk[n] & M1};
    olo = mfma16(a, u32x2{v[0], v[1]}, olo);
    ohi = mfma16(a, u32x2{v[2], v[3]}, ohi);
    if constexpr (n + WV < TPW) vt[n + WV] = LD128(rv, voff, (n + WV) * 4096, AUX_NT);
    if constexpr (n == TPW - WV - 4) {
      if (w == 2 || (w == 3 && j < 64)) granule_wait<1, 0>(rws, rdyoff, rdy, TL, TH);
    }
    if constexpr (n == TPW - WV - 3) {
      st = LD64(rws, stoff, 0, AUX_SC1);
      hg[0] = LD64(rws, hgoff, 0, AUX_SC1);
      hg[1] = LD64(rws, hgoff, 4 * NB * FF * 8, AUX_SC1);
    }
    if constexpr (n == TPW - WV) {
      lds_await(&lds[L_CS], NWAVE);
      float M = lds[L_MX + g];
#pragma unroll
      for (int ww = 1; ww < NWAVE; ++ww) M = fmaxf(M, lds[L_MX + ww * 4 + g]);
      fw = __builtin_amdgcn_exp2f(lds[L_MX + w * 4 + g] - M);
    }
    if constexpr (n == TPW - 3) {
      if (w == 2) {
        if (__builtin_expect(!__all(max(st.x, st.y) != EMPTY), 0)) {
#pragma nounroll
          for (u32 spin = 0; spin < SPIN_CAP; ++spin) {
            __builtin_amdgcn_s_sleep(2);
            asm volatile("" : "+v"(stoff)::"memory");
            st = LD64(rws, stoff, 0, AUX_SC1);
            if (__all(max(st.x, st.y) != EMPTY)) break;
          }
        }
        float Mwg = lds[L_MX + (lane & 3)];
#pragma unroll
        for (int ww = 1; ww < NWAVE; ++ww) Mwg = fmaxf(Mwg, lds[L_MX + ww * 4 + (lane & 3)]);
        float M = as_f(st.x);
        M = fmaxf(M, dpp<ROR4>(M));
        M = fmaxf(M, dpp<ROR8>(M));
        M = xrow_max(M);
        float L = as_f(st.y) * __builtin_amdgcn_exp2f(as_f(st.x) - M);
        L += dpp<ROR4>(L);
        L += dpp<ROR8>(L);
        L = xrow_sum(L) * 0.5f;
        if (lane < 4) lds[L_SC + lane] = __builtin_amdgcn_exp2f(Mwg - M) / L;
      }
    }
    pin();
  });
  STAMP(5);
  *(f32x4*)&lds[L_RED + w * 512 + lane * 8] = olo * fw;
  *(f32x4*)&lds[L_RED + w * 512 + lane * 8 + 4] = ohi * fw;
  wg_barrier();
  if (tid < 128) {
    f32x4 v = *(const f32x4*)&lds[L_RED + tid * 4];
#pragma unroll
    for (int ww = 1; ww < NWAVE; ++ww) v += *(const f32x4*)&lds[L_RED + ww * 512 + tid * 4];
    v *= lds[L_SC + (tid >> 5)];
    const u32x4 o = {noempty(v[0]), noempty(v[1]), noempty(v[2]), noempty(v[3])};
    ST128(o, rws, WS_AO + chunk * 2048 + tid * 16, AUX_SC1);
  }
  STAMP(6);
  {
    f32x4 glo = {0.f, 0.f, 0.f, 0.f}, ghi = {0.f, 0.f, 0.f, 0.f};
    {
      granule_wait<2, 4 * NB * FF * 8>(rws, hgoff, hg, TL, TH);
      float gt = g_val(hg[0]), up = g_val(hg[1]);
      gt += dpp<QX1>(gt);
      up += dpp<QX1>(up);
      gt += dpp<QX2>(gt);
      up += dpp<QX2>(up);
      const float hv = rbf(sigmoidf_(rbf(gt))) * rbf(up);
      const u32 hb = w < 7 ? cvt_pk(hv, hv) : 0u;
      const u32x2 ah = {hb & M0, hb & M1};
      glo = mfma16(ah, u32x2{wdt[0], wdt[1]}, glo);
      ghi = mfma16(ah, u32x2{wdt[2], wdt[3]}, ghi);
    }
    const float gate = rbf(sigmoidf_(rbf(lds[L_GT + 2 * g] + lds[L_GT + 2 * g + 1])));
    const float og = rbf(1.0f - gate);
    u32 oo = WS_AO + (u32)(((r16 >> 2) * 8 + x8) * 8 + 2 * u_) * 2048u + (u32)(rs_ * 64 + 8 * w + 2 * g) * 4u;
    u32x2 o0 = {EMPTY, EMPTY}, o1 = {EMPTY, EMPTY};
    bool need = true;
#pragma nounroll
    for (u32 spin = 0; spin < SPIN_CAP; ++spin) {
      asm volatile("" : "+v"(oo)::"memory");
      if (need) {
        o0 = LD64(rws, oo, 0, AUX_SC1);
        o1 = LD64(rws, oo + 2048u, 0, AUX_SC1);
        need = max(max(o0.x, o0.y), max(o1.x, o1.y)) == EMPTY;
      }
      if (!__any(need)) break;
    }
    STAMP(7);
    float e0 = as_f(o0.x) + as_f(o1.x), e1v = as_f(o0.y) + as_f(o1.y);
    e0 += dpp<QX1>(e0);
    e1v += dpp<QX1>(e1v);
    e0 += dpp<QX2>(e0);
    e1v += dpp<QX2>(e1v);
    const u32 p0 = cvt_pk(e0, e0), p1 = cvt_pk(e1v, e1v);
    f32x4 alo = {0.f, 0.f, 0.f, 0.f}, ahi = {0.f, 0.f, 0.f, 0.f};
    alo = mfma16(u32x2{p0 & M0, p0 & M1}, u32x2{wot[0][0], wot[0][1]}, alo);
    ahi = mfma16(u32x2{p0 & M0, p0 & M1}, u32x2{wot[0][2], wot[0][3]}, ahi);
    alo = mfma16(u32x2{p1 & M0, p1 & M1}, u32x2{wot[1][0], wot[1][1]}, alo);
    ahi = mfma16(u32x2{p1 & M0, p1 & M1}, u32x2{wot[1][2], wot[1][3]}, ahi);
    *(f32x4*)&lds[L_RED2 + w * 512 + lane * 8] = alo * gate + glo * og;
    *(f32x4*)&lds[L_RED2 + w * 512 + lane * 8 + 4] = ahi * gate + ghi * og;
  }
  wg_barrier();
  if (tid < 128) {
    f32x4 v = *(const f32x4*)&lds[L_RED2 + tid * 4];
#pragma unroll
    for (int ww = 1; ww < NWAVE; ++ww) v += *(const f32x4*)&lds[L_RED2 + ww * 512 + tid * 4];
    const u32x4 o = {noempty(v[0]), noempty(v[1]), noempty(v[2]), noempty(v[3])};
    const int line = tid >> 3;
    ST128(o, rws, WS_BP + (((line >> 2) * 16 + cs * 4 + (line & 3)) * 64 + ia) * 128 + (tid & 7) * 16, AUX_SC1);
  }
  STAMP(8);
  if (j < 64) {
    u32 bo = WS_BP + (u32)(j * 64 + w * 8 + (lane >> 3)) * 128u + (u32)(lane & 7) * 16u;
    u32x4 pv = {EMPTY, EMPTY, EMPTY, EMPTY};
    bool need = true;
#pragma nounroll
    for (u32 spin = 0; spin < SPIN_CAP; ++spin) {
      asm volatile("" : "+v"(bo)::"memory");
      if (need) {
        pv = LD128(rws, bo, 0, AUX_SC1);
        need = max(max(pv[0], pv[1]), max(pv[2], pv[3])) == EMPTY;
      }
      if (!__any(need)) break;
    }
    f32x4 v = __builtin_bit_cast(f32x4, pv);
#pragma unroll
    for (int e = 0; e < 4; ++e) {
      float t = v[e];
      t += dpp<ROR8>(t);
      v[e] = xrow_sum(t);
    }
    if (lane < 8) *(f32x4*)&lds[L_RED + w * 32 + lane * 4] = v;
    wg_barrier();
    if (tid < 16) {
      float a0 = lds[L_RED + 2 * tid], a1 = lds[L_RED + 2 * tid + 1];
#pragma unroll
      for (int ww = 1; ww < NWAVE; ++ww) {
        a0 += lds[L_RED + ww * 32 + 2 * tid];
        a1 += lds[L_RED + ww * 32 + 2 * tid + 1];
      }
      *(u32*)(out + (j >> 4) * DM + (j & 15) * 32 + 2 * tid) = cvt_pk(a0, a1);
      if (j == 0 && tid == 0) {
        const u32x2 ne = {(u32)e1, (u32)(e1 >> 32)};
        ST64(ne, rws, WS_EPOCH, AUX_SC1);
      }
    }
    STAMP(9);
  }
  STAMP_FLUSH();
}

}

extern "C" void kernel_launch(void* const* d_in, const int* in_sizes, int n_in, void* d_out, int out_size, void* d_ws,
                              size_t ws_size, hipStream_t stream) {
  (void)in_sizes; (void)n_in; (void)out_size; (void)ws_size;
  fused_decode<<<NWG, NTHR, 0, stream>>>((const u16*)d_in[0], (const u16*)d_in[1], (const u16*)d_in[2], (const u16*)d_in[3],
                                         (const u16*)d_in[4], (const u16*)d_in[6], (const u16*)d_in[7], (const u16*)d_in[8],
                                         (const u16*)d_in[9], (u16*)d_out, (unsigned char*)d_ws);
}
```

```cpp
#include <hip/hip_runtime.h>
#include <hip/hip_bf16.h>
#include <stdint.h>

namespace {

typedef unsigned int u32;
typedef unsigned short u16;
typedef unsigned long long u64;
typedef u32 u32x2 __attribute__((ext_vector_type(2)));
typedef u32 u32x4 __attribute__((ext_vector_type(4)));
typedef float f32x4 __attribute__((ext_vector_type(4)));
typedef short s16x4 __attribute__((ext_vector_type(4)));
typedef short s16x8 __attribute__((ext_vector_type(8)));
typedef __bf16 bf16x2 __attribute__((ext_vector_type(2)));

constexpr int NB = 4, DM = 512, HQ = 32, HKV = 8, SK = 8192, DH = 128, FF = 1792;
constexpr int NWG = 256, NTHR = 512, NWAVE = 8;
constexpr int TPW = 32;

#define WK 5
#define WV 8
#define WV1 5
#define POLL_SLEEP 2
#define PSTEP (WK + 2)
#define WOLAG 6
#define PLAG 2
#define SSTEP 16
#define QSTEP 27

static_assert(QSTEP < TPW && QSTEP + WK + 1 >= TPW && QSTEP + WK + 1 < TPW + WV1, "q check rides on the first V loads");
static_assert(PSTEP > WK && PSTEP + PLAG + WK <= SSTEP && SSTEP + WK + 1 <= QSTEP, "schedule");
static_assert(WV1 <= WV && WV - WV1 <= 14 && TPW - WV > 4, "schedule");

constexpr u32 WS_EPOCH = 0;
constexpr u32 WS_RDY = 1024;
constexpr u32 WS_QG = 4096;
constexpr u32 WS_HG = WS_QG + 4 * NB * 4096 * 8;
constexpr u32 WS_AS = WS_HG + 2 * 4 * NB * FF * 8;
constexpr u32 WS_AO = WS_AS + NWG * 32;
constexpr u32 WS_BP = WS_AO + NWG * 2048;
constexpr u32 WS_END = WS_BP + 64 * 64 * 128;
constexpr u32 WS_DBG = 4u << 20;

constexpr u32 EMPTY = 0xFFFFFFFFu;
constexpr u32 OOB = 0x7F000000u;
constexpr int AUX_SC1 = 16, AUX_NT = 2;
constexpr u32 SPIN_CAP = 1u << 19;

constexpr int L_GT = 0;
constexpr int L_SC = 8;
constexpr int L_CQ = 12;
constexpr int L_CS = 13;
constexpr int L_CP = 14;
constexpr int L_MX = 16;
constexpr int L_LS = 48;
constexpr int L_QS = 80;
constexpr int L_RED = 336;
constexpr int L_RED2 = L_RED + 4096;
constexpr int L_TOTAL = L_RED2 + 4096;

__device__ __forceinline__ float as_f(u32 x) { return __builtin_bit_cast(float, x); }
__device__ __forceinline__ u32 as_u(float x) { return __builtin_bit_cast(u32, x); }
__device__ __forceinline__ u32 cvt_pk(float lo, float hi) {
  u32 r;
  asm("v_cvt_pk_bf16_f32 %0, %1, %2" : "=v"(r) : "v"(lo), "v"(hi));
  return r;
}
__device__ __forceinline__ float rbf(float x) { return as_f(cvt_pk(x, x) & 0xFFFF0000u); }
__device__ __forceinline__ float dot2(u32 a, u32 b, float c) {
  return __builtin_amdgcn_fdot2_f32_bf16(__builtin_bit_cast(bf16x2, a), __builtin_bit_cast(bf16x2, b), c, false);
}
template <int CTRL>
__device__ __forceinline__ float dpp(float x) {
  return as_f(__builtin_amdgcn_mov_dpp((int)as_u(x), CTRL, 0xf, 0xf, true));
}
constexpr int QP0 = 0x00, QP1 = 0x55, QP2 = 0xAA, QP3 = 0xFF, QX1 = 0xB1, QX2 = 0x4E;
constexpr int ROR1 = 0x121, ROR2 = 0x122, ROR4 = 0x124, ROR8 = 0x128;
__device__ __forceinline__ float xrow_max(float x) {
  auto s = __builtin_amdgcn_permlane16_swap(as_u(x), as_u(x), false, false);
  x = fmaxf(as_f(s[0]), as_f(s[1]));
  auto t = __builtin_amdgcn_permlane32_swap(as_u(x), as_u(x), false, false);
  return fmaxf(as_f(t[0]), as_f(t[1]));
}
__device__ __forceinline__ float xrow_sum(float x) {
  auto s = __builtin_amdgcn_permlane16_swap(as_u(x), as_u(x), false, false);
  x = as_f(s[0]) + as_f(s[1]);
  auto t = __builtin_amdgcn_permlane32_swap(as_u(x), as_u(x), false, false);
  return as_f(t[0]) + as_f(t[1]);
}
__device__ __forceinline__ float row_sum16(float v) {
  v += dpp<ROR8>(v);
  v += dpp<ROR4>(v);
  v += dpp<ROR2>(v);
  v += dpp<ROR1>(v);
  return v;
}
__device__ __forceinline__ f32x4 mfma32(u32x4 a, u32x4 b, f32x4 c) {
  return __builtin_amdgcn_mfma_f32_16x16x32_bf16(__builtin_bit_cast(s16x8, a), __builtin_bit_cast(s16x8, b), c, 0, 0, 0);
}
__device__ __forceinline__ f32x4 mfma16(u32x2 a, u32x2 b, f32x4 c) {
  return __builtin_amdgcn_mfma_f32_16x16x16bf16_1k(__builtin_bit_cast(s16x4, a), __builtin_bit_cast(s16x4, b), c, 0, 0, 0);
}
template <int N>
__device__ __forceinline__ void waitvm() {
  asm volatile("s_waitcnt vmcnt(%0)" ::"i"(N) : "memory");
}
__device__ __forceinline__ void wg_barrier() { asm volatile("s_waitcnt lgkmcnt(0)\n\ts_barrier" ::: "memory"); }
__device__ __forceinline__ void pin() { __builtin_amdgcn_sched_barrier(0); }
__device__ __forceinline__ u32 noempty(float v) { return min(as_u(v), 0xFFFFFFFEu); }
__device__ __forceinline__ float sigmoidf_(float x) { return 1.0f / (1.0f + __expf(-x)); }

__device__ __forceinline__ u32 lds_arrive(float* c) {
  asm volatile("s_waitcnt lgkmcnt(0)" ::: "memory");
  return __hip_atomic_fetch_add((u32*)c, 1u, __ATOMIC_RELAXED, __HIP_MEMORY_SCOPE_WORKGROUP);
}
__device__ __forceinline__ void lds_await(float* c, u32 n) {
  while (__hip_atomic_load((u32*)c, __ATOMIC_RELAXED, __HIP_MEMORY_SCOPE_WORKGROUP) < n) __builtin_amdgcn_s_sleep(1);
  asm volatile("" ::: "memory");
}
template <int V>
struct ic {
  static constexpr int value = V;
};
template <int I, int N, class F>
__device__ __forceinline__ void static_for(F&& f) {
  if constexpr (I < N) {
    f(ic<I>{});
    static_for<I + 1, N>(f);
  }
}

typedef __amdgpu_buffer_rsrc_t rsrc_t;
__device__ __forceinline__ rsrc_t mk(const void* p, u32 bytes) {
  return __builtin_amdgcn_make_buffer_rsrc(const_cast<void*>(p), 0, bytes, 0x00020000);
}
#define LD128(r, v, s, aux) __builtin_amdgcn_raw_buffer_load_b128((r), (v), (s), (aux))
#define LD64(r, v, s, aux) __builtin_amdgcn_raw_buffer_load_b64((r), (v), (s), (aux))
#define ST128(x, r, v, aux) __builtin_amdgcn_raw_buffer_store_b128((x), (r), (v), 0, (aux))
#define ST64(x, r, v, aux) __builtin_amdgcn_raw_buffer_store_b64((x), (r), (v), 0, (aux))

#define STAMP(i)
#define STAMP_FLUSH()

__device__ __forceinline__ u32x2 mk_granule(float v, u32 TL, u32 TH) {
  const u32 u = as_u(v);
  return u32x2{((u + 0x7Fu + ((u >> 8) & 1u)) >> 8) | TL, TH};
}
__device__ __forceinline__ bool g_ok(u32x2 g, u32 TL, u32 TH) { return ((g.x ^ TL) < 0x1000000u) & (g.y == TH); }
__device__ __forceinline__ float g_val(u32x2 g) { return as_f(g.x << 8); }
template <int N, u32 STRIDE>
__device__ __forceinline__ void granule_wait(rsrc_t rs, u32 off, u32x2 (&g)[N], u32 TL, u32 TH) {
  bool ok = true;
#pragma unroll
  for (int k = 0; k < N; ++k) ok &= g_ok(g[k], TL, TH);
  if (__builtin_expect(!__all(ok), 0)) {
#pragma nounroll
    for (u32 spin = 0; spin < SPIN_CAP; ++spin) {
      __builtin_amdgcn_s_sleep(4);
      asm volatile("" : "+v"(off)::"memory");
      ok = true;
#pragma unroll
      for (int k = 0; k < N; ++k) g[k] = LD64(rs, off, k * STRIDE, AUX_SC1);
#pragma unroll
      for (int k = 0; k < N; ++k) ok &= g_ok(g[k], TL, TH);
      if (__all(ok)) break;
    }
  }
}
__device__ __forceinline__ void proj_tile(u32x4 ra, u32x4 rb, const u32 (&xp)[4], float (&o)[8]) {
  float acc[32];
#pragma unroll
  for (int cp = 0; cp < 4; ++cp) {
    const u32 lo = __builtin_amdgcn_perm(rb[cp], ra[cp], 0x05040100u), hi = __builtin_amdgcn_perm(rb[cp], ra[cp], 0x07060302u);
#pragma unroll
    for (int b = 0; b < 4; ++b) {
      acc[b * 8 + 2 * cp] = dot2(lo, xp[b], 0.f);
      acc[b * 8 + 2 * cp + 1] = dot2(hi, xp[b], 0.f);
    }
  }
#pragma unroll
  for (int i = 0; i < 16; ++i) {
    auto t = __builtin_amdgcn_permlane32_swap(as_u(acc[i]), as_u(acc[i + 16]), false, false);
    acc[i] = as_f(t[0]) + as_f(t[1]);
  }
#pragma unroll
  for (int i = 0; i < 8; ++i) {
    auto t = __builtin_amdgcn_permlane16_swap(as_u(acc[i]), as_u(acc[i + 8]), false, false);
    const float v = as_f(t[0]) + as_f(t[1]);
    o[i] = v + dpp<ROR8>(v);
  }
}

__global__ void __launch_bounds__(NTHR, 2)
fused_decode(const u16* x, const u16* kc, const u16* vc, const u16* wq, const u16* wo, const u16* wg, const u16* wu,
             const u16* wd, const u16* gw, u16* out, unsigned char* ws) {
  __shared__ __attribute__((aligned(16))) float lds[L_TOTAL];
  const int tid = threadIdx.x, lane = tid & 63;
  const int w = __builtin_amdgcn_readfirstlane(tid >> 6);
  const int j = blockIdx.x, x8 = j & 7, i32 = j >> 3;
  const int g = lane >> 4, r16 = lane & 15;
  STAMP(0);
  if (tid < 3) ((u32*)lds)[L_CQ + tid] = 0u;

  const int chunk = x8 * 32 + i32;
  const int ab = chunk >> 6, akvh = (chunk >> 3) & 7;
  const int kq = j & 3, qcb = j >> 2;
  const bool has_glu = j < 224;
  const int gmat = j >= 112 ? 1 : 0, gcb = (j - 112 * gmat) >> 2;
  const int rs_ = i32 >> 2, cs = i32 & 3, ia = x8 * 8 + rs_;

  const rsrc_t rws = mk(ws, WS_END);
  const rsrc_t rk = mk(kc, (u32)NB * HKV * SK * DH * 2), rv = mk(vc, (u32)NB * HKV * SK * DH * 2);

  const u32x2 ep = LD64(rws, WS_EPOCH, 0, AUX_SC1);
  const int prow = kq * 128 + 16 * w + 2 * (lane >> 3);
  u32x4 wa, wb, ga, gb;
  u32 xl;
  {
    const rsrc_t rq = mk(wq, DM * HQ * DH * 2), rg = mk(gmat ? wu : wg, DM * FF * 2);
    const u32 qo = (u32)(prow * 4096 + qcb * 64 + (lane & 7) * 8) * 2u;
    const u32 go = has_glu ? (u32)(prow * FF + gcb * 64 + (lane & 7) * 8) * 2u : OOB;
    wa = LD128(rq, qo, 0, AUX_NT);
    wb = LD128(rq, qo, 4096 * 2, AUX_NT);
    ga = LD128(rg, go, 0, AUX_NT);
    gb = LD128(rg, go, FF * 2, AUX_NT);
    xl = *(const u32*)(x + ((lane >> 3) & 3) * DM + kq * 128 + 16 * w + 2 * (lane & 7));
  }
  pin();
  const u32 tbase = (u32)chunk * 262144u + (u32)((w >> 2) * 128 + (w & 3)) * 1024u;
  const u32 koff = tbase + (u32)((r16 >> 2) * 256 + g * 64 + (r16 & 3) * 16);
  const u32 voff = tbase + (u32)lane * 16u;
  u32x4 kt[TPW];
#pragma unroll
  for (int i = 0; i < WK; ++i) kt[i] = LD128(rk, koff, i * 4096, AUX_NT);
  pin();
  wg_barrier();
  {
    const u32x4 e4 = {EMPTY, EMPTY, EMPTY, EMPTY};
    const int line = (w & 1) * 8 + (lane >> 3);
    const u32 oa = WS_AO + chunk * 2048 + (w & 1) * 1024 + lane * 16;
    const u32 ob = WS_BP + (((line >> 2) * 16 + cs * 4 + (line & 3)) * 64 + ia) * 128 + (lane & 7) * 16;
    const u32 os = (w == 4 && lane < 2) ? WS_AS + chunk * 32 + lane * 16 : OOB;
    ST128(e4, rws, w < 2 ? oa : (w < 4 ? ob : os), AUX_SC1);
  }
  pin();

  const int u_ = r16 & 3;
  const u32 M0 = u_ == 0 ? 0xFFFFu : (u_ == 1 ? 0xFFFF0000u : 0u), M1 = u_ == 2 ? 0xFFFFu : (u_ == 3 ? 0xFFFF0000u : 0u);
  u32 qgoff = WS_QG + (u32)(((lane >> 5) * 4 + ab) * 4096 + akvh * 512 + w * 64 + 2 * (lane & 31)) * 8u;
  const u32 hgoff = WS_HG + (u32)((u_ * 4 + (r16 >> 2)) * FF + 28 * ia + 4 * (w < 7 ? w : 6) + g) * 8u;
  u32 rdyoff;
  {
    const int l5 = lane & 31, cA = ((l5 >> 3) * 8 + x8) * 8 + (l5 & 7);
    const int cS = (chunk & ~7) + (lane & 7);
    const int cx = lane < 32 ? cA : cS;
    const int jA = (cx & 31) * 8 + (cx >> 5);
    const int jB = (lane & 7) + 8 * ((lane >> 3) * 4 + ((j & 15) >> 2));
    rdyoff = (w == 2 || (w == 3 && j < 64)) ? WS_RDY + (u32)(w == 3 ? jB : jA) * 8u : OOB;
  }
  u64 e1 = 0;
  u32 TL = 0, TH = 0;
  u32x4 qg[2];
  u32x4 wot[2], wdt;
  u32x2 hg[2], rdy[1], xg, gwv;
  static_for<WK, TPW>([&](auto ii) {
    constexpr int i = ii.value;
    if constexpr (i == PSTEP) {
      u32 xp[4];
#pragma unroll
      for (int b = 0; b < 4; ++b) xp[b] = __builtin_amdgcn_ds_bpermute((b * 8 + (lane >> 3)) * 4, xl);
      float o[8];
      proj_tile(wa, wb, xp, o);
      float* dst = &lds[L_RED2 + w * 256 + g * 64 + (r16 & 7) * 8];
      if (r16 < 8) {
        *(f32x4*)dst = f32x4{o[0], o[1], o[2], o[3]};
        *(f32x4*)(dst + 4) = f32x4{o[4], o[5], o[6], o[7]};
      }
      proj_tile(ga, gb, xp, o);
      if (r16 < 8) {
        *(f32x4*)(dst + 2048) = f32x4{o[0], o[1], o[2], o[3]};
        *(f32x4*)(dst + 2052) = f32x4{o[4], o[5], o[6], o[7]};
      }
      e1 = (((u64)__builtin_amdgcn_readfirstlane(ep.y) << 32) | (u32)__builtin_amdgcn_readfirstlane(ep.x)) + 1ull;
      TL = (u32)e1 << 24;
      TH = (u32)(e1 >> 8);
      STAMP(1);
      waitvm<PSTEP - WK>();
      if (lane == 0) lds_arrive(&lds[L_CP]);
      pin();
    }
    if constexpr (i == PSTEP + PLAG) {
      lds_await(&lds[L_CP], NWAVE);
      const float* src = &lds[L_RED2 + (w >> 2) * 2048 + (w & 3) * 64 + lane];
      float v = src[0];
#pragma unroll
      for (int ww = 1; ww < NWAVE; ++ww) v += src[ww * 256];
      const u32 off = w < 4 ? WS_QG + (u32)((kq * 4 + w) * 4096 + qcb * 64 + lane) * 8u
                            : (has_glu ? WS_HG + (u32)(((gmat * 4 + kq) * 4 + (w - 4)) * FF + gcb * 64 + lane) * 8u : OOB);
      ST64(mk_granule(v, TL, TH), rws, off, AUX_SC1);
      if (tid == 0) ST64((u32x2{TL, TH}), rws, WS_RDY + (u32)j * 8u, AUX_SC1);
      pin();
    }
    waitvm<WK - 1 + (i < PSTEP ? 1 : 0) + ((i >= PSTEP + PLAG && i < PSTEP + PLAG + WK) ? 1 : 0) +
           ((i > SSTEP && i <= SSTEP + WK) ? 2 : 0) + (i > QSTEP ? 3 : 0)>();
    kt[i] = LD128(rk, koff, i * 4096, AUX_NT);
    if constexpr (i == SSTEP) {
      xg = *(const u32x2*)(x + w * 256 + lane * 4);
      gwv = *(const u32x2*)(gw + (w & 1) * 256 + lane * 4);
    }
    if constexpr (i == SSTEP + WK + 1) {
      float t = rbf(as_f(xg.x << 16) * as_f(gwv.x << 16)) + rbf(as_f(xg.x & 0xFFFF0000u) * as_f(gwv.x & 0xFFFF0000u));
      t += rbf(as_f(xg.y << 16) * as_f(gwv.y << 16)) + rbf(as_f(xg.y & 0xFFFF0000u) * as_f(gwv.y & 0xFFFF0000u));
      t = xrow_sum(row_sum16(t));
      if (lane == 0) lds[L_GT + w] = t;
    }
    if constexpr (i == QSTEP) {
      qg[0] = LD128(rws, qgoff, 0, AUX_SC1);
      qg[1] = LD128(rws, qgoff, 2 * NB * 4096 * 8, AUX_SC1);
      rdy[0] = LD64(rws, rdyoff, 0, AUX_SC1);
    }
    pin();
  });
  u32x4 vt[TPW];
  static_for<0, WV1>([&](auto nn) {
    constexpr int n = nn.value;
    waitvm<WK - 1 + (n + TPW <= QSTEP + WK ? 3 : 0)>();
    vt[n] = LD128(rv, voff, n * 4096, AUX_NT);
    if constexpr (n + TPW == QSTEP + WK + 1) {
      auto q_ok = [&]() {
        bool ok = true;
#pragma unroll
        for (int k = 0; k < 2; ++k) ok &= g_ok(u32x2{qg[k][0], qg[k][1]}, TL, TH) & g_ok(u32x2{qg[k][2], qg[k][3]}, TL, TH);
        return ok;
      };
      if (__builtin_expect(!__all(q_ok()), 0)) {
#pragma nounroll
        for (u32 spin = 0; spin < SPIN_CAP; ++spin) {
          __builtin_amdgcn_s_sleep(4);
          asm volatile("" : "+v"(qgoff)::"memory");
          qg[0] = LD128(rws, qgoff, 0, AUX_SC1);
          qg[1] = LD128(rws, qgoff, 2 * NB * 4096 * 8, AUX_SC1);
          if (__all(q_ok())) break;
        }
      }
      const float h0 = as_f(qg[0][0] << 8) + as_f(qg[1][0] << 8), h1 = as_f(qg[0][2] << 8) + as_f(qg[1][2] << 8);
      auto t0 = __builtin_amdgcn_permlane32_swap(as_u(h0), as_u(h0), false, false);
      auto t1 = __builtin_amdgcn_permlane32_swap(as_u(h1), as_u(h1), false, false);
      if (lane < 32) ((u32*)lds)[L_QS + w * 32 + lane] = cvt_pk(as_f(t0[0]) + as_f(t0[1]), as_f(t1[0]) + as_f(t1[1]));
      if (lane == 0) lds_arrive(&lds[L_CQ]);
    }
    pin();
  });
  STAMP(2);
  lds_await(&lds[L_CQ], NWAVE);
  const u32x4 qf = *(const u32x4*)((const u16*)&lds[L_QS] + (r16 >> 2) * 128 + g * 32 + (r16 & 3) * 8);
  STAMP(3);

  float s[TPW];
  static_for<0, TPW / 4>([&](auto ii) {
    constexpr int i = ii.value * 4;
    f32x4 d[4];
#pragma unroll
    for (int t = 0; t < 4; ++t) d[t] = mfma32(kt[i + t], qf, f32x4{0.f, 0.f, 0.f, 0.f});
#pragma unroll
    for (int t = 0; t < 4; ++t) s[i + t] = (dpp<QP0>(d[t][0]) + dpp<QP1>(d[t][1])) + (dpp<QP2>(d[t][2]) + dpp<QP3>(d[t][3]));
    if constexpr (i >= 4) {
#pragma unroll
      for (int t = 0; t < 2; ++t)
        if ((i - 4) / 2 + t + WV1 < WV) vt[(i - 4) / 2 + t + WV1] = LD128(rv, voff, ((i - 4) / 2 + t + WV1) * 4096, AUX_NT);
    }
    pin();
  });
  STAMP(4);
  constexpr float C2 = 0.088388347648318440f * 1.4426950408889634f;
  float mw = s[0];
#pragma unroll
  for (int i = 1; i < TPW; ++i) mw = fmaxf(mw, s[i]);
  mw = xrow_max(mw) * C2;
  float lw = 0.f;
  u32 pk[TPW];
#pragma unroll
  for (int i = 0; i < TPW; ++i) {
    const float p = __builtin_amdgcn_exp2f(fmaf(s[i], C2, -mw));
    lw += p;
    pk[i] = cvt_pk(p, p);
  }
  lw = xrow_sum(lw);
  if (g == 0 && u_ == 0) {
    lds[L_MX + w * 4 + (r16 >> 2)] = mw;
    lds[L_LS + w * 4 + (r16 >> 2)] = lw;
  }
  {
    u32 old = 0;
    if (lane == 0) old = lds_arrive(&lds[L_CS]);
    const bool last = __builtin_amdgcn_readfirstlane(old) == NWAVE - 1;
    float M = 0.f, L = 0.f;
    if (last) {
      asm volatile("" ::: "memory");
      const int hd = lane & 3;
      M = lds[L_MX + hd];
#pragma unroll
      for (int ww = 1; ww < NWAVE; ++ww) M = fmaxf(M, lds[L_MX + ww * 4 + hd]);
#pragma unroll
      for (int ww = 0; ww < NWAVE; ++ww) L += lds[L_LS + ww * 4 + hd] * __builtin_amdgcn_exp2f(lds[L_MX + ww * 4 + hd] - M);
    }
    ST64((u32x2{noempty(M), noempty(L)}), rws, (last && lane < 4) ? WS_AS + chunk * 32 + lane * 8 : OOB, AUX_SC1);
  }
  pin();

  f32x4 olo = {0.f, 0.f, 0.f, 0.f}, ohi = {0.f, 0.f, 0.f, 0.f};
  float fw = 0.f;
  u32x2 st;
  u32 stoff = w == 2 ? WS_AS + (u32)(chunk & ~7) * 32u + (u32)(lane & 31) * 8u : OOB;
  static_for<0, TPW>([&](auto nn) {
    constexpr int n = nn.value;
    const u32x4 v = vt[n];
    const u32x2 a = {pk[n] & M0, pk[n] & M1};
    olo = mfma16(a, u32x2{v[0], v[1]}, olo);
    ohi = mfma16(a, u32x2{v[2], v[3]}, ohi);
    if constexpr (n + WV < TPW) vt[n + WV] = LD128(rv, voff, (n + WV) * 4096, AUX_NT);
    if constexpr (n == TPW - WV - 4) {
      if (w == 2 || (w == 3 && j < 64)) granule_wait<1, 0>(rws, rdyoff, rdy, TL, TH);
      asm volatile("" ::: "memory");
    }
    if constexpr (n == TPW - WV - 3) {
      st = LD64(rws, stoff, 0, AUX_SC1);
      hg[0] = LD64(rws, hgoff, 0, AUX_SC1);
      hg[1] = LD64(rws, hgoff, 4 * NB * FF * 8, AUX_SC1);
    }
    if constexpr (n == TPW - WV + WOLAG) {
      const rsrc_t ro = mk(wo, HQ * DH * DM * 2), rd = mk(wd, FF * DM * 2);
      const u32 oo = (u32)((x8 * 512 + rs_ * 64 + 8 * w + 2 * g) * DM + cs * 128 + r16 * 8) * 2u;
      wot[0] = LD128(ro, oo, 0, AUX_NT);
      wot[1] = LD128(ro, oo, DM * 2, AUX_NT);
      wdt = LD128(rd, w < 7 ? (u32)((28 * ia + 4 * w + g) * DM + cs * 128 + r16 * 8) * 2u : OOB, 0, AUX_NT);
    }
    if constexpr (n == TPW - WV) {
      lds_await(&lds[L_CS], NWAVE);
      float M = lds[L_MX + g];
#pragma unroll
      for (int ww = 1; ww < NWAVE; ++ww) M = fmaxf(M, lds[L_MX + ww * 4 + g]);
      fw = __builtin_amdgcn_exp2f(lds[L_MX + w * 4 + g] - M);
    }
    if constexpr (n == TPW - 3) {
      if (w == 2) {
        if (__builtin_expect(!__all(max(st.x, st.y) != EMPTY), 0)) {
#pragma nounroll
          for (u32 spin = 0; spin < SPIN_CAP; ++spin) {
            __builtin_amdgcn_s_sleep(2);
            asm volatile("" : "+v"(stoff)::"memory");
            st = LD64(rws, stoff, 0, AUX_SC1);
            if (__all(max(st.x, st.y) != EMPTY)) break;
          }
        }
        float Mwg = lds[L_MX + (lane & 3)];
#pragma unroll
        for (int ww = 1; ww < NWAVE; ++ww) Mwg = fmaxf(Mwg, lds[L_MX + ww * 4 + (lane & 3)]);
        float M = as_f(st.x);
        M = fmaxf(M, dpp<ROR4>(M));
        M = fmaxf(M, dpp<ROR8>(M));
        M = xrow_max(M);
        float L = as_f(st.y) * __builtin_amdgcn_exp2f(as_f(st.x) - M);
        L += dpp<ROR4>(L);
        L += dpp<ROR8>(L);
        L = xrow_sum(L) * 0.5f;
        if (lane < 4) lds[L_SC + lane] = __builtin_amdgcn_exp2f(Mwg - M) / L;
      }
    }
    pin();
  });
  STAMP(5);
  *(f32x4*)&lds[L_RED + w * 512 + lane * 8] = olo * fw;
  *(f32x4*)&lds[L_RED + w * 512 + lane * 8 + 4] = ohi * fw;
  wg_barrier();
  if (tid < 128) {
    f32x4 v = *(const f32x4*)&lds[L_RED + tid * 4];
#pragma unroll
    for (int ww = 1; ww < NWAVE; ++ww) v += *(const f32x4*)&lds[L_RED + ww * 512 + tid * 4];
    v *= lds[L_SC + (tid >> 5)];
    const u32x4 o = {noempty(v[0]), noempty(v[1]), noempty(v[2]), noempty(v[3])};
    ST128(o, rws, WS_AO + chunk * 2048 + tid * 16, AUX_SC1);
  }
  STAMP(6);
  {
    f32x4 glo = {0.f, 0.f, 0.f, 0.f}, ghi = {0.f, 0.f, 0.f, 0.f};
    {
      granule_wait<2, 4 * NB * FF * 8>(rws, hgoff, hg, TL, TH);
      float gt = g_val(hg[0]), up = g_val(hg[1]);
      gt += dpp<QX1>(gt);
      up += dpp<QX1>(up);
      gt += dpp<QX2>(gt);
      up += dpp<QX2>(up);
      const float hv = rbf(sigmoidf_(rbf(gt))) * rbf(up);
      const u32 hb = w < 7 ? cvt_pk(hv, hv) : 0u;
      const u32x2 ah = {hb & M0, hb & M1};
      glo = mfma16(ah, u32x2{wdt[0], wdt[1]}, glo);
      ghi = mfma16(ah, u32x2{wdt[2], wdt[3]}, ghi);
    }
    const float gate = rbf(sigmoidf_(rbf(lds[L_GT + 2 * g] + lds[L_GT + 2 * g + 1])));
    const float og = rbf(1.0f - gate);
    u32 oo = WS_AO + (u32)(((r16 >> 2) * 8 + x8) * 8 + 2 * u_) * 2048u + (u32)(rs_ * 64 + 8 * w + 2 * g) * 4u;
    u32x2 o0 = {EMPTY, EMPTY}, o1 = {EMPTY, EMPTY};
    bool need = true;
#pragma nounroll
    for (u32 spin = 0; spin < SPIN_CAP; ++spin) {
      asm volatile("" : "+v"(oo)::"memory");
      if (need) {
        o0 = LD64(rws, oo, 0, AUX_SC1);
        o1 = LD64(rws, oo + 2048u, 0, AUX_SC1);
        need = max(max(o0.x, o0.y), max(o1.x, o1.y)) == EMPTY;
      }
      if (!__any(need)) break;
      __builtin_amdgcn_s_sleep(POLL_SLEEP);
    }
    STAMP(7);
    float e0 = as_f(o0.x) + as_f(o1.x), e1v = as_f(o0.y) + as_f(o1.y);
    e0 += dpp<QX1>(e0);
    e1v += dpp<QX1>(e1v);
    e0 += dpp<QX2>(e0);
    e1v += dpp<QX2>(e1v);
    const u32 p0 = cvt_pk(e0, e0), p1 = cvt_pk(e1v, e1v);
    f32x4 alo = {0.f, 0.f, 0.f, 0.f}, ahi = {0.f, 0.f, 0.f, 0.f};
    alo = mfma16(u32x2{p0 & M0, p0 & M1}, u32x2{wot[0][0], wot[0][1]}, alo);
    ahi = mfma16(u32x2{p0 & M0, p0 & M1}, u32x2{wot[0][2], wot[0][3]}, ahi);
    alo = mfma16(u32x2{p1 & M0, p1 & M1}, u32x2{wot[1][0], wot[1][1]}, alo);
    ahi = mfma16(u32x2{p1 & M0, p1 & M1}, u32x2{wot[1][2], wot[1][3]}, ahi);
    *(f32x4*)&lds[L_RED2 + w * 512 + lane * 8] = alo * gate + glo * og;
    *(f32x4*)&lds[L_RED2 + w * 512 + lane * 8 + 4] = ahi * gate + ghi * og;
  }
  wg_barrier();
  if (tid < 128) {
    f32x4 v = *(const f32x4*)&lds[L_RED2 + tid * 4];
#pragma unroll
    for (int ww = 1; ww < NWAVE; ++ww) v += *(const f32x4*)&lds[L_RED2 + ww * 512 + tid * 4];
    const u32x4 o = {noempty(v[0]), noempty(v[1]), noempty(v[2]), noempty(v[3])};
    const int line = tid >> 3;
    ST128(o, rws, WS_BP + (((line >> 2) * 16 + cs * 4 + (line & 3)) * 64 + ia) * 128 + (tid & 7) * 16, AUX_SC1);
  }
  STAMP(8);
  if (j < 64) {
    u32 bo = WS_BP + (u32)(j * 64 + w * 8 + (lane >> 3)) * 128u + (u32)(lane & 7) * 16u;
    u32x4 pv = {EMPTY, EMPTY, EMPTY, EMPTY};
    bool need = true;
#pragma nounroll
    for (u32 spin = 0; spin < SPIN_CAP; ++spin) {
      asm volatile("" : "+v"(bo)::"memory");
      if (need) {
        pv = LD128(rws, bo, 0, AUX_SC1);
        need = max(max(pv[0], pv[1]), max(pv[2], pv[3])) == EMPTY;
      }
      if (!__any(need)) break;
      __builtin_amdgcn_s_sleep(1);
    }
    f32x4 v = __builtin_bit_cast(f32x4, pv);
#pragma unroll
    for (int e = 0; e < 4; ++e) {
      float t = v[e];
      t += dpp<ROR8>(t);
      v[e] = xrow_sum(t);
    }
    if (lane < 8) *(f32x4*)&lds[L_RED + w * 32 + lane * 4] = v;
    wg_barrier();
    if (tid < 16) {
      float a0 = lds[L_RED + 2 * tid], a1 = lds[L_RED + 2 * tid + 1];
#pragma unroll
      for (int ww = 1; ww < NWAVE; ++ww) {
        a0 += lds[L_RED + ww * 32 + 2 * tid];
        a1 += lds[L_RED + ww * 32 + 2 * tid + 1];
      }
      *(u32*)(out + (j >> 4) * DM + (j & 15) * 32 + 2 * tid) = cvt_pk(a0, a1);
      if (j == 0 && tid == 0) {
        const u32x2 ne = {(u32)e1, (u32)(e1 >> 32)};
        ST64(ne, rws, WS_EPOCH, AUX_SC1);
      }
    }
    STAMP(9);
  }
  STAMP_FLUSH();
}

}

extern "C" void kernel_launch(void* const* d_in, const int* in_sizes, int n_in, void* d_out, int out_size, void* d_ws,
                              size_t ws_size, hipStream_t stream) {
  (void)in_sizes; (void)n_in; (void)out_size; (void)ws_size;
  fused_decode<<<NWG, NTHR, 0, stream>>>((const u16*)d_in[0], (const u16*)d_in[1], (const u16*)d_in[2], (const u16*)d_in[3],
                                         (const u16*)d_in[4], (const u16*)d_in[6], (const u16*)d_in[7], (const u16*)d_in[8],
                                         (const u16*)d_in[9], (u16*)d_out, (unsigned char*)d_ws);
}
```

```cpp
#include <hip/hip_runtime.h>
#include <hip/hip_bf16.h>
#include <stdint.h>

namespace {

typedef unsigned int u32;
typedef unsigned short u16;
typedef unsigned long long u64;
typedef u32 u32x2 __attribute__((ext_vector_type(2)));
typedef u32 u32x4 __attribute__((ext_vector_type(4)));
typedef float f32x4 __attribute__((ext_vector_type(4)));
typedef short s16x4 __attribute__((ext_vector_type(4)));
typedef short s16x8 __attribute__((ext_vector_type(8)));
typedef __bf16 bf16x2 __attribute__((ext_vector_type(2)));

constexpr int NB = 4, DM = 512, HQ = 32, HKV = 8, SK = 8192, DH = 128, FF = 1792;
constexpr int NWG = 256, NTHR = 512, NWAVE = 8;
constexpr int TPW = 32;

#define WK 5
#define WV 8
#define WV1 5
#define POLL_SLEEP 2
#define PSTEP (WK + 2)
#define WOLAG 6
#define PLAG 2
#define SSTEP 16
#define QSTEP 27

static_assert(QSTEP < TPW && QSTEP + WK + 1 >= TPW && QSTEP + WK + 1 < TPW + WV1, "q check rides on the first V loads");
static_assert(PSTEP > WK && PSTEP + PLAG + WK <= SSTEP && SSTEP + WK + 1 <= QSTEP, "schedule");
static_assert(WV1 <= WV && WV - WV1 <= 14 && TPW - WV > 4, "schedule");

constexpr u32 WS_EPOCH = 0;
constexpr u32 WS_RDY = 1024;
constexpr u32 WS_QG = 4096;
constexpr u32 WS_HG = WS_QG + 4 * NB * 4096 * 8;
constexpr u32 WS_AS = WS_HG + 2 * 4 * NB * FF * 8;
constexpr u32 WS_AO = WS_AS + NWG * 32;
constexpr u32 WS_BP = WS_AO + NWG * 2048;
constexpr u32 WS_END = WS_BP + 64 * 64 * 128;
constexpr u32 WS_DBG = 4u << 20;

constexpr u32 EMPTY = 0xFFFFFFFFu;
constexpr u32 OOB = 0x7F000000u;
constexpr int AUX_SC1 = 16, AUX_NT = 2;
constexpr u32 SPIN_CAP = 1u << 19;

constexpr int L_GT = 0;
constexpr int L_SC = 8;
constexpr int L_CQ = 12;
constexpr int L_CS = 13;
constexpr int L_CP = 14;
constexpr int L_MX = 16;
constexpr int L_LS = 48;
constexpr int L_QS = 80;
constexpr int L_RED = 336;
constexpr int L_RED2 = L_RED + 4096;
constexpr int L_TOTAL = L_RED2 + 4096;

__device__ __forceinline__ float as_f(u32 x) { return __builtin_bit_cast(float, x); }
__device__ __forceinline__ u32 as_u(float x) { return __builtin_bit_cast(u32, x); }
__device__ __forceinline__ u32 cvt_pk(float lo, float hi) {
  u32 r;
  asm("v_cvt_pk_bf16_f32 %0, %1, %2" : "=v"(r) : "v"(lo), "v"(hi));
  return r;
}
__device__ __forceinline__ float rbf(float x) { return as_f(cvt_pk(x, x) & 0xFFFF0000u); }
__device__ __forceinline__ float dot2(u32 a, u32 b, float c) {
  return __builtin_amdgcn_fdot2_f32_bf16(__builtin_bit_cast(bf16x2, a), __builtin_bit_cast(bf16x2, b), c, false);
}
template <int CTRL>
__device__ __forceinline__ float dpp(float x) {
  return as_f(__builtin_amdgcn_mov_dpp((int)as_u(x), CTRL, 0xf, 0xf, true));
}
constexpr int QP0 = 0x00, QP1 = 0x55, QP2 = 0xAA, QP3 = 0xFF, QX1 = 0xB1, QX2 = 0x4E;
constexpr int ROR1 = 0x121, ROR2 = 0x122, ROR4 = 0x124, ROR8 = 0x128;
__device__ __forceinline__ float xrow_max(float x) {
  auto s = __builtin_amdgcn_permlane16_swap(as_u(x), as_u(x), false, false);
  x = fmaxf(as_f(s[0]), as_f(s[1]));
  auto t = __builtin_amdgcn_permlane32_swap(as_u(x), as_u(x), false, false);
  return fmaxf(as_f(t[0]), as_f(t[1]));
}
__device__ __forceinline__ float xrow_sum(float x) {
  auto s = __builtin_amdgcn_permlane16_swap(as_u(x), as_u(x), false, false);
  x = as_f(s[0]) + as_f(s[1]);
  auto t = __builtin_amdgcn_permlane32_swap(as_u(x), as_u(x), false, false);
  return as_f(t[0]) + as_f(t[1]);
}
__device__ __forceinline__ float row_sum16(float v) {
  v += dpp<ROR8>(v);
  v += dpp<ROR4>(v);
  v += dpp<ROR2>(v);
  v += dpp<ROR1>(v);
  return v;
}
__device__ __forceinline__ f32x4 mfma32(u32x4 a, u32x4 b, f32x4 c) {
  return __builtin_amdgcn_mfma_f32_16x16x32_bf16(__builtin_bit_cast(s16x8, a), __builtin_bit_cast(s16x8, b), c, 0, 0, 0);
}
__device__ __forceinline__ f32x4 mfma16(u32x2 a, u32x2 b, f32x4 c) {
  return __builtin_amdgcn_mfma_f32_16x16x16bf16_1k(__builtin_bit_cast(s16x4, a), __builtin_bit_cast(s16x4, b), c, 0, 0, 0);
}
template <int N>
__device__ __forceinline__ void waitvm() {
  asm volatile("s_waitcnt vmcnt(%0)" ::"i"(N) : "memory");
}
__device__ __forceinline__ void wg_barrier() { asm volatile("s_waitcnt lgkmcnt(0)\n\ts_barrier" ::: "memory"); }
__device__ __forceinline__ void pin() { __builtin_amdgcn_sched_barrier(0); }
__device__ __forceinline__ u32 noempty(float v) { return min(as_u(v), 0xFFFFFFFEu); }
__device__ __forceinline__ float sigmoidf_(float x) { return 1.0f / (1.0f + __expf(-x)); }

__device__ __forceinline__ u32 lds_arrive(float* c) {
  asm volatile("s_waitcnt lgkmcnt(0)" ::: "memory");
  return __hip_atomic_fetch_add((u32*)c, 1u, __ATOMIC_RELAXED, __HIP_MEMORY_SCOPE_WORKGROUP);
}
__device__ __forceinline__ void lds_await(float* c, u32 n) {
  while (__hip_atomic_load((u32*)c, __ATOMIC_RELAXED, __HIP_MEMORY_SCOPE_WORKGROUP) < n) __builtin_amdgcn_s_sleep(1);
  asm volatile("" ::: "memory");
}
template <int V>
struct ic {
  static constexpr int value = V;
};
template <int I, int N, class F>
__device__ __forceinline__ void static_for(F&& f) {
  if constexpr (I < N) {
    f(ic<I>{});
    static_for<I + 1, N>(f);
  }
}

typedef __amdgpu_buffer_rsrc_t rsrc_t;
__device__ __forceinline__ rsrc_t mk(const void* p, u32 bytes) {
  return __builtin_amdgcn_make_buffer_rsrc(const_cast<void*>(p), 0, bytes, 0x00020000);
}
#define LD128(r, v, s, aux) __builtin_amdgcn_raw_buffer_load_b128((r), (v), (s), (aux))
#define LD64(r, v, s, aux) __builtin_amdgcn_raw_buffer_load_b64((r), (v), (s), (aux))
#define ST128(x, r, v, aux) __builtin_amdgcn_raw_buffer_store_b128((x), (r), (v), 0, (aux))
#define ST64(x, r, v, aux) __builtin_amdgcn_raw_buffer_store_b64((x), (r), (v), 0, (aux))

#define STAMP(i)
#define STAMP_FLUSH()

__device__ __forceinline__ u32x2 mk_granule(float v, u32 TL, u32 TH) {
  const u32 u = as_u(v);
  return u32x2{((u + 0x7Fu + ((u >> 8) & 1u)) >> 8) | TL, TH};
}
__device__ __forceinline__ bool g_ok(u32x2 g, u32 TL, u32 TH) { return ((g.x ^ TL) < 0x1000000u) & (g.y == TH); }
__device__ __forceinline__ float g_val(u32x2 g) { return as_f(g.x << 8); }
template <int N, u32 STRIDE>
__device__ __forceinline__ void granule_wait(rsrc_t rs, u32 off, u32x2 (&g)[N], u32 TL, u32 TH) {
  bool ok = true;
#pragma unroll
  for (int k = 0; k < N; ++k) ok &= g_ok(g[k], TL, TH);
  if (__builtin_expect(!__all(ok), 0)) {
#pragma nounroll
    for (u32 spin = 0; spin < SPIN_CAP; ++spin) {
      __builtin_amdgcn_s_sleep(4);
      asm volatile("" : "+v"(off)::"memory");
      ok = true;
#pragma unroll
      for (int k = 0; k < N; ++k) g[k] = LD64(rs, off, k * STRIDE, AUX_SC1);
#pragma unroll
      for (int k = 0; k < N; ++k) ok &= g_ok(g[k], TL, TH);
      if (__all(ok)) break;
    }
  }
}
__device__ __forceinline__ void proj_tile(u32x4 ra, u32x4 rb, const u32 (&xp)[4], float (&o)[8]) {
  float acc[32];
#pragma unroll
  for (int cp = 0; cp < 4; ++cp) {
    const u32 lo = __builtin_amdgcn_perm(rb[cp], ra[cp], 0x05040100u), hi = __builtin_amdgcn_perm(rb[cp], ra[cp], 0x07060302u);
#pragma unroll
    for (int b = 0; b < 4; ++b) {
      acc[b * 8 + 2 * cp] = dot2(lo, xp[b], 0.f);
      acc[b * 8 + 2 * cp + 1] = dot2(hi, xp[b], 0.f);
    }
  }
#pragma unroll
  for (int i = 0; i < 16; ++i) {
    auto t = __builtin_amdgcn_permlane32_swap(as_u(acc[i]), as_u(acc[i + 16]), false, false);
    acc[i] = as_f(t[0]) + as_f(t[1]);
  }
#pragma unroll
  for (int i = 0; i < 8; ++i) {
    auto t = __builtin_amdgcn_permlane16_swap(as_u(acc[i]), as_u(acc[i + 8]), false, false);
    const float v = as_f(t[0]) + as_f(t[1]);
    o[i] = v + dpp<ROR8>(v);
  }
}

__global__ void __launch_bounds__(NTHR, 2)
fused_decode(const u16* x, const u16* kc, const u16* vc, const u16* wq, const u16* wo, const u16* wg, const u16* wu,
             const u16* wd, const u16* gw, u16* out, unsigned char* ws) {
  __shared__ __attribute__((aligned(16))) float lds[L_TOTAL];
  const int tid = threadIdx.x, lane = tid & 63;
  const int w = __builtin_amdgcn_readfirstlane(tid >> 6);
  const int j = blockIdx.x, x8 = j & 7, i32 = j >> 3;
  const int g = lane >> 4, r16 = lane & 15;
  STAMP(0);
  if (tid < 3) ((u32*)lds)[L_CQ + tid] = 0u;

  const int chunk = x8 * 32 + i32;
  const int ab = chunk >> 6, akvh = (chunk >> 3) & 7;
  const int kq = j & 3, qcb = j >> 2;
  const bool has_glu = j < 224;
  const int gmat = j >= 112 ? 1 : 0, gcb = (j - 112 * gmat) >> 2;
  const int rs_ = i32 >> 2, cs = i32 & 3, ia = x8 * 8 + rs_;

  const rsrc_t rws = mk(ws, WS_END);
  const rsrc_t rk = mk(kc, (u32)NB * HKV * SK * DH * 2), rv = mk(vc, (u32)NB * HKV * SK * DH * 2);

  const u32x2 ep = LD64(rws, WS_EPOCH, 0, AUX_SC1);
  const int prow = kq * 128 + 16 * w + 2 * (lane >> 3);
  u32x4 wa, wb, ga, gb;
  u32 xl;
  {
    const rsrc_t rq = mk(wq, DM * HQ * DH * 2), rg = mk(gmat ? wu : wg, DM * FF * 2);
    const u32 qo = (u32)(prow * 4096 + qcb * 64 + (lane & 7) * 8) * 2u;
    const u32 go = has_glu ? (u32)(prow * FF + gcb * 64 + (lane & 7) * 8) * 2u : OOB;
    wa = LD128(rq, qo, 0, AUX_NT);
    wb = LD128(rq, qo, 4096 * 2, AUX_NT);
    ga = LD128(rg, go, 0, AUX_NT);
    gb = LD128(rg, go, FF * 2, AUX_NT);
    xl = *(const u32*)(x + ((lane >> 3) & 3) * DM + kq * 128 + 16 * w + 2 * (lane & 7));
  }
  pin();
  const u32 tbase = (u32)chunk * 262144u + (u32)((w >> 2) * 128 + (w & 3)) * 1024u;
  const u32 koff = tbase + (u32)((r16 >> 2) * 256 + g * 64 + (r16 & 3) * 16);
  const u32 voff = tbase + (u32)lane * 16u;
  u32x4 kt[TPW];
#pragma unroll
  for (int i = 0; i < WK; ++i) kt[i] = LD128(rk, koff, i * 4096, AUX_NT);
  pin();
  wg_barrier();
  {
    const u32x4 e4 = {EMPTY, EMPTY, EMPTY, EMPTY};
    const int line = (w & 1) * 8 + (lane >> 3);
    const u32 oa = WS_AO + chunk * 2048 + (w & 1) * 1024 + lane * 16;
    const u32 ob = WS_BP + (((line >> 2) * 16 + cs * 4 + (line & 3)) * 64 + ia) * 128 + (lane & 7) * 16;
    const u32 os = (w == 4 && lane < 2) ? WS_AS + chunk * 32 + lane * 16 : OOB;
    ST128(e4, rws, w < 2 ? oa : (w < 4 ? ob : os), AUX_SC1);
  }
  pin();

  const int u_ = r16 & 3;
  const u32 M0 = u_ == 0 ? 0xFFFFu : (u_ == 1 ? 0xFFFF0000u : 0u), M1 = u_ == 2 ? 0xFFFFu : (u_ == 3 ? 0xFFFF0000u : 0u);
  u32 qgoff = WS_QG + (u32)(((lane >> 5) * 4 + ab) * 4096 + akvh * 512 + w * 64 + 2 * (lane & 31)) * 8u;
  const u32 hgoff = WS_HG + (u32)((u_ * 4 + (r16 >> 2)) * FF + 28 * ia + 4 * (w < 7 ? w : 6) + g) * 8u;
  u32 rdyoff;
  {
    const int l5 = lane & 31, cA = ((l5 >> 3) * 8 + x8) * 8 + (l5 & 7);
    const int cS = (chunk & ~7) + (lane & 7);
    const int cx = lane < 32 ? cA : cS;
    const int jA = (cx & 31) * 8 + (cx >> 5);
    const int jB = (lane & 7) + 8 * ((lane >> 3) * 4 + ((j & 15) >> 2));
    rdyoff = (w == 2 || (w == 3 && j < 64)) ? WS_RDY + (u32)(w == 3 ? jB : jA) * 8u : OOB;
  }
  u64 e1 = 0;
  u32 TL = 0, TH = 0;
  u32x4 qg[2];
  u32x4 wot[2], wdt;
  u32x2 hg[2], rdy[1], xg, gwv;
  const u32 woff = (u32)((x8 * 512 + rs_ * 64 + 8 * w + 2 * g) * DM + cs * 128 + r16 * 8) * 2u;
  const u32 doff = w < 7 ? (u32)((28 * ia + 4 * w + g) * DM + cs * 128 + r16 * 8) * 2u : OOB;
  static_for<WK, TPW>([&](auto ii) {
    constexpr int i = ii.value;
    if constexpr (i == PSTEP) {
      u32 xp[4];
#pragma unroll
      for (int b = 0; b < 4; ++b) xp[b] = __builtin_amdgcn_ds_bpermute((b * 8 + (lane >> 3)) * 4, xl);
      float o[8];
      proj_tile(wa, wb, xp, o);
      float* dst = &lds[L_RED2 + w * 256 + g * 64 + (r16 & 7) * 8];
      if (r16 < 8) {
        *(f32x4*)dst = f32x4{o[0], o[1], o[2], o[3]};
        *(f32x4*)(dst + 4) = f32x4{o[4], o[5], o[6], o[7]};
      }
      proj_tile(ga, gb, xp, o);
      if (r16 < 8) {
        *(f32x4*)(dst + 2048) = f32x4{o[0], o[1], o[2], o[3]};
        *(f32x4*)(dst + 2052) = f32x4{o[4], o[5], o[6], o[7]};
      }
      e1 = (((u64)__builtin_amdgcn_readfirstlane(ep.y) << 32) | (u32)__builtin_amdgcn_readfirstlane(ep.x)) + 1ull;
      TL = (u32)e1 << 24;
      TH = (u32)(e1 >> 8);
      STAMP(1);
      waitvm<PSTEP - WK>();
      if (lane == 0) lds_arrive(&lds[L_CP]);
      pin();
    }
    if constexpr (i == PSTEP + PLAG) {
      lds_await(&lds[L_CP], NWAVE);
      const float* src = &lds[L_RED2 + (w >> 2) * 2048 + (w & 3) * 64 + lane];
      float v = src[0];
#pragma unroll
      for (int ww = 1; ww < NWAVE; ++ww) v += src[ww * 256];
      const u32 off = w < 4 ? WS_QG + (u32)((kq * 4 + w) * 4096 + qcb * 64 + lane) * 8u
                            : (has_glu ? WS_HG + (u32)(((gmat * 4 + kq) * 4 + (w - 4)) * FF + gcb * 64 + lane) * 8u : OOB);
      ST64(mk_granule(v, TL, TH), rws, off, AUX_SC1);
      if (tid == 0) ST64((u32x2{TL, TH}), rws, WS_RDY + (u32)j * 8u, AUX_SC1);
      pin();
    }
    waitvm<WK - 1 + (i < PSTEP ? 1 : 0) + ((i >= PSTEP + PLAG && i < PSTEP + PLAG + WK) ? 1 : 0) +
           ((i > SSTEP && i <= SSTEP + WK) ? 2 : 0) + (i > QSTEP ? 3 : 0)>();
    kt[i] = LD128(rk, koff, i * 4096, AUX_NT);
    if constexpr (i == SSTEP) {
      xg = *(const u32x2*)(x + w * 256 + lane * 4);
      gwv = *(const u32x2*)(gw + (w & 1) * 256 + lane * 4);
    }
    if constexpr (i == SSTEP + WK + 1) {
      float t = rbf(as_f(xg.x << 16) * as_f(gwv.x << 16)) + rbf(as_f(xg.x & 0xFFFF0000u) * as_f(gwv.x & 0xFFFF0000u));
      t += rbf(as_f(xg.y << 16) * as_f(gwv.y << 16)) + rbf(as_f(xg.y & 0xFFFF0000u) * as_f(gwv.y & 0xFFFF0000u));
      t = xrow_sum(row_sum16(t));
      if (lane == 0) lds[L_GT + w] = t;
    }
    if constexpr (i == QSTEP) {
      qg[0] = LD128(rws, qgoff, 0, AUX_SC1);
      qg[1] = LD128(rws, qgoff, 2 * NB * 4096 * 8, AUX_SC1);
      rdy[0] = LD64(rws, rdyoff, 0, AUX_SC1);
    }
    pin();
  });
  u32x4 vt[TPW];
  static_for<0, WV1>([&](auto nn) {
    constexpr int n = nn.value;
    waitvm<WK - 1 + (n + TPW <= QSTEP + WK ? 3 : 0)>();
    vt[n] = LD128(rv, voff, n * 4096, AUX_NT);
    if constexpr (n + TPW == QSTEP + WK + 1) {
      auto q_ok = [&]() {
        bool ok = true;
#pragma unroll
        for (int k = 0; k < 2; ++k) ok &= g_ok(u32x2{qg[k][0], qg[k][1]}, TL, TH) & g_ok(u32x2{qg[k][2], qg[k][3]}, TL, TH);
        return ok;
      };
      if (__builtin_expect(!__all(q_ok()), 0)) {
#pragma nounroll
        for (u32 spin = 0; spin < SPIN_CAP; ++spin) {
          __builtin_amdgcn_s_sleep(4);
          asm volatile("" : "+v"(qgoff)::"memory");
          qg[0] = LD128(rws, qgoff, 0, AUX_SC1);
          qg[1] = LD128(rws, qgoff, 2 * NB * 4096 * 8, AUX_SC1);
          if (__all(q_ok())) break;
        }
      }
      const float h0 = as_f(qg[0][0] << 8) + as_f(qg[1][0] << 8), h1 = as_f(qg[0][2] << 8) + as_f(qg[1][2] << 8);
      auto t0 = __builtin_amdgcn_permlane32_swap(as_u(h0), as_u(h0), false, false);
      auto t1 = __builtin_amdgcn_permlane32_swap(as_u(h1), as_u(h1), false, false);
      if (lane < 32) ((u32*)lds)[L_QS + w * 32 + lane] = cvt_pk(as_f(t0[0]) + as_f(t0[1]), as_f(t1[0]) + as_f(t1[1]));
      if (lane == 0) lds_arrive(&lds[L_CQ]);
    }
    pin();
  });
  STAMP(2);
  lds_await(&lds[L_CQ], NWAVE);
  const u32x4 qf = *(const u32x4*)((const u16*)&lds[L_QS] + (r16 >> 2) * 128 + g * 32 + (r16 & 3) * 8);
  STAMP(3);

  float s[TPW];
  static_for<0, TPW / 4>([&](auto ii) {
    constexpr int i = ii.value * 4;
    f32x4 d[4];
#pragma unroll
    for (int t = 0; t < 4; ++t) d[t] = mfma32(kt[i + t], qf, f32x4{0.f, 0.f, 0.f, 0.f});
#pragma unroll
    for (int t = 0; t < 4; ++t) s[i + t] = (dpp<QP0>(d[t][0]) + dpp<QP1>(d[t][1])) + (dpp<QP2>(d[t][2]) + dpp<QP3>(d[t][3]));
    if constexpr (i >= 4) {
#pragma unroll
      for (int t = 0; t < 2; ++t)
        if ((i - 4) / 2 + t + WV1 < WV) vt[(i - 4) / 2 + t + WV1] = LD128(rv, voff, ((i - 4) / 2 + t + WV1) * 4096, AUX_NT);
    }
    pin();
  });
  STAMP(4);
  constexpr float C2 = 0.088388347648318440f * 1.4426950408889634f;
  float mw = s[0];
#pragma unroll
  for (int i = 1; i < TPW; ++i) mw = fmaxf(mw, s[i]);
  mw = xrow_max(mw) * C2;
  float lw = 0.f;
  u32 pk[TPW];
#pragma unroll
  for (int i = 0; i < TPW; ++i) {
    const float p = __builtin_amdgcn_exp2f(fmaf(s[i], C2, -mw));
    lw += p;
    pk[i] = cvt_pk(p, p);
  }
  lw = xrow_sum(lw);
  if (g == 0 && u_ == 0) {
    lds[L_MX + w * 4 + (r16 >> 2)] = mw;
    lds[L_LS + w * 4 + (r16 >> 2)] = lw;
  }
  {
    u32 old = 0;
    if (lane == 0) old = lds_arrive(&lds[L_CS]);
    const bool last = __builtin_amdgcn_readfirstlane(old) == NWAVE - 1;
    float M = 0.f, L = 0.f;
    if (last) {
      asm volatile("" ::: "memory");
      const int hd = lane & 3;
      M = lds[L_MX + hd];
#pragma unroll
      for (int ww = 1; ww < NWAVE; ++ww) M = fmaxf(M, lds[L_MX + ww * 4 + hd]);
#pragma unroll
      for (int ww = 0; ww < NWAVE; ++ww) L += lds[L_LS + ww * 4 + hd] * __builtin_amdgcn_exp2f(lds[L_MX + ww * 4 + hd] - M);
    }
    ST64((u32x2{noempty(M), noempty(L)}), rws, (last && lane < 4) ? WS_AS + chunk * 32 + lane * 8 : OOB, AUX_SC1);
  }
  pin();

  f32x4 olo = {0.f, 0.f, 0.f, 0.f}, ohi = {0.f, 0.f, 0.f, 0.f};
  float fw = 0.f;
  u32x2 st;
  u32 stoff = w == 2 ? WS_AS + (u32)(chunk & ~7) * 32u + (u32)(lane & 31) * 8u : OOB;
  static_for<0, TPW>([&](auto nn) {
    constexpr int n = nn.value;
    const u32x4 v = vt[n];
    const u32x2 a = {pk[n] & M0, pk[n] & M1};
    olo = mfma16(a, u32x2{v[0], v[1]}, olo);
    ohi = mfma16(a, u32x2{v[2], v[3]}, ohi);
    if constexpr (n + WV < TPW) vt[n + WV] = LD128(rv, voff, (n + WV) * 4096, AUX_NT);
    if constexpr (n == TPW - WV - 4) {
      if (w == 2 || (w == 3 && j < 64)) granule_wait<1, 0>(rws, rdyoff, rdy, TL, TH);
      asm volatile("" ::: "memory");
    }
    if constexpr (n == TPW - WV - 3) {
      st = LD64(rws, stoff, 0, AUX_SC1);
      hg[0] = LD64(rws, hgoff, 0, AUX_SC1);
      hg[1] = LD64(rws, hgoff, 4 * NB * FF * 8, AUX_SC1);
    }
    if constexpr (n == TPW - WV + WOLAG) {
      const rsrc_t ro = mk(wo, HQ * DH * DM * 2), rd = mk(wd, FF * DM * 2);
      wot[0] = LD128(ro, woff, 0, AUX_NT);
      wot[1] = LD128(ro, woff, DM * 2, AUX_NT);
      wdt = LD128(rd, doff, 0, AUX_NT);
    }
    if constexpr (n == TPW - WV) {
      lds_await(&lds[L_CS], NWAVE);
      float M = lds[L_MX + g];
#pragma unroll
      for (int ww = 1; ww < NWAVE; ++ww) M = fmaxf(M, lds[L_MX + ww * 4 + g]);
      fw = __builtin_amdgcn_exp2f(lds[L_MX + w * 4 + g] - M);
    }
    if constexpr (n == TPW - 3) {
      if (w == 2) {
        if (__builtin_expect(!__all(max(st.x, st.y) != EMPTY), 0)) {
#pragma nounroll
          for (u32 spin = 0; spin < SPIN_CAP; ++spin) {
            __builtin_amdgcn_s_sleep(2);
            asm volatile("" : "+v"(stoff)::"memory");
            st = LD64(rws, stoff, 0, AUX_SC1);
            if (__all(max(st.x, st.y) != EMPTY)) break;
          }
        }
        float Mwg = lds[L_MX + (lane & 3)];
#pragma unroll
        for (int ww = 1; ww < NWAVE; ++ww) Mwg = fmaxf(Mwg, lds[L_MX + ww * 4 + (lane & 3)]);
        float M = as_f(st.x);
        M = fmaxf(M, dpp<ROR4>(M));
        M = fmaxf(M, dpp<ROR8>(M));
        M = xrow_max(M);
        float L = as_f(st.y) * __builtin_amdgcn_exp2f(as_f(st.x) - M);
        L += dpp<ROR4>(L);
        L += dpp<ROR8>(L);
        L = xrow_sum(L) * 0.5f;
        if (lane < 4) lds[L_SC + lane] = __builtin_amdgcn_exp2f(Mwg - M) / L;
      }
    }
    pin();
  });
  STAMP(5);
  *(f32x4*)&lds[L_RED + w * 512 + lane * 8] = olo * fw;
  *(f32x4*)&lds[L_RED + w * 512 + lane * 8 + 4] = ohi * fw;
  wg_barrier();
  if (tid < 128) {
    f32x4 v = *(const f32x4*)&lds[L_RED + tid * 4];
#pragma unroll
    for (int ww = 1; ww < NWAVE; ++ww) v += *(const f32x4*)&lds[L_RED + ww * 512 + tid * 4];
    v *= lds[L_SC + (tid >> 5)];
    const u32x4 o = {noempty(v[0]), noempty(v[1]), noempty(v[2]), noempty(v[3])};
    ST128(o, rws, WS_AO + chunk * 2048 + tid * 16, AUX_SC1);
  }
  STAMP(6);
  {
    f32x4 glo = {0.f, 0.f, 0.f, 0.f}, ghi = {0.f, 0.f, 0.f, 0.f};
    {
      granule_wait<2, 4 * NB * FF * 8>(rws, hgoff, hg, TL, TH);
      float gt = g_val(hg[0]), up = g_val(hg[1]);
      gt += dpp<QX1>(gt);
      up += dpp<QX1>(up);
      gt += dpp<QX2>(gt);
      up += dpp<QX2>(up);
      const float hv = rbf(sigmoidf_(rbf(gt))) * rbf(up);
      const u32 hb = w < 7 ? cvt_pk(hv, hv) : 0u;
      const u32x2 ah = {hb & M0, hb & M1};
      glo = mfma16(ah, u32x2{wdt[0], wdt[1]}, glo);
      ghi = mfma16(ah, u32x2{wdt[2], wdt[3]}, ghi);
    }
    const float gate = rbf(sigmoidf_(rbf(lds[L_GT + 2 * g] + lds[L_GT + 2 * g + 1])));
    const float og = rbf(1.0f - gate);
    u32 oo = WS_AO + (u32)(((r16 >> 2) * 8 + x8) * 8 + 2 * u_) * 2048u + (u32)(rs_ * 64 + 8 * w + 2 * g) * 4u;
    u32x2 o0 = {EMPTY, EMPTY}, o1 = {EMPTY, EMPTY};
    bool need = true;
#pragma nounroll
    for (u32 spin = 0; spin < SPIN_CAP; ++spin) {
      asm volatile("" : "+v"(oo)::"memory");
      if (need) {
        o0 = LD64(rws, oo, 0, AUX_SC1);
        o1 = LD64(rws, oo + 2048u, 0, AUX_SC1);
        need = max(max(o0.x, o0.y), max(o1.x, o1.y)) == EMPTY;
      }
      if (!__any(need)) break;
      __builtin_amdgcn_s_sleep(POLL_SLEEP);
    }
    STAMP(7);
    float e0 = as_f(o0.x) + as_f(o1.x), e1v = as_f(o0.y) + as_f(o1.y);
    e0 += dpp<QX1>(e0);
    e1v += dpp<QX1>(e1v);
    e0 += dpp<QX2>(e0);
    e1v += dpp<QX2>(e1v);
    const u32 p0 = cvt_pk(e0, e0), p1 = cvt_pk(e1v, e1v);
    f32x4 alo = {0.f, 0.f, 0.f, 0.f}, ahi = {0.f, 0.f, 0.f, 0.f};
    alo = mfma16(u32x2{p0 & M0, p0 & M1}, u32x2{wot[0][0], wot[0][1]}, alo);
    ahi = mfma16(u32x2{p0 & M0, p0 & M1}, u32x2{wot[0][2], wot[0][3]}, ahi);
    alo = mfma16(u32x2{p1 & M0, p1 & M1}, u32x2{wot[1][0], wot[1][1]}, alo);
    ahi = mfma16(u32x2{p1 & M0, p1 & M1}, u32x2{wot[1][2], wot[1][3]}, ahi);
    *(f32x4*)&lds[L_RED2 + w * 512 + lane * 8] = alo * gate + glo * og;
    *(f32x4*)&lds[L_RED2 + w * 512 + lane * 8 + 4] = ahi * gate + ghi * og;
  }
  wg_barrier();
  if (tid < 128) {
    f32x4 v = *(const f32x4*)&lds[L_RED2 + tid * 4];
#pragma unroll
    for (int ww = 1; ww < NWAVE; ++ww) v += *(const f32x4*)&lds[L_RED2 + ww * 512 + tid * 4];
    const u32x4 o = {noempty(v[0]), noempty(v[1]), noempty(v[2]), noempty(v[3])};
    const int line = tid >> 3;
    ST128(o, rws, WS_BP + (((line >> 2) * 16 + cs * 4 + (line & 3)) * 64 + ia) * 128 + (tid & 7) * 16, AUX_SC1);
  }
  STAMP(8);
  if (j < 64) {
    u32 bo = WS_BP + (u32)(j * 64 + w * 8 + (lane >> 3)) * 128u + (u32)(lane & 7) * 16u;
    u32x4 pv = {EMPTY, EMPTY, EMPTY, EMPTY};
    bool need = true;
#pragma nounroll
    for (u32 spin = 0; spin < SPIN_CAP; ++spin) {
      asm volatile("" : "+v"(bo)::"memory");
      if (need) {
        pv = LD128(rws, bo, 0, AUX_SC1);
        need = max(max(pv[0], pv[1]), max(pv[2], pv[3])) == EMPTY;
      }
      if (!__any(need)) break;
      __builtin_amdgcn_s_sleep(1);
    }
    f32x4 v = __builtin_bit_cast(f32x4, pv);
#pragma unroll
    for (int e = 0; e < 4; ++e) {
      float t = v[e];
      t += dpp<ROR8>(t);
      v[e] = xrow_sum(t);
    }
    if (lane < 8) *(f32x4*)&lds[L_RED + w * 32 + lane * 4] = v;
    wg_barrier();
    if (tid < 16) {
      float a0 = lds[L_RED + 2 * tid], a1 = lds[L_RED + 2 * tid + 1];
#pragma unroll
      for (int ww = 1; ww < NWAVE; ++ww) {
        a0 += lds[L_RED + ww * 32 + 2 * tid];
        a1 += lds[L_RED + ww * 32 + 2 * tid + 1];
      }
      *(u32*)(out + (j >> 4) * DM + (j & 15) * 32 + 2 * tid) = cvt_pk(a0, a1);
      if (j == 0 && tid == 0) {
        const u32x2 ne = {(u32)e1, (u32)(e1 >> 32)};
        ST64(ne, rws, WS_EPOCH, AUX_SC1);
      }
    }
    STAMP(9);
  }
  STAMP_FLUSH();
}

}

extern "C" void kernel_launch(void* const* d_in, const int* in_sizes, int n_in, void* d_out, int out_size, void* d_ws,
                              size_t ws_size, hipStream_t stream) {
  (void)in_sizes; (void)n_in; (void)out_size; (void)ws_size;
  fused_decode<<<NWG, NTHR, 0, stream>>>((const u16*)d_in[0], (const u16*)d_in[1], (const u16*)d_in[2], (const u16*)d_in[3],
                                         (const u16*)d_in[4], (const u16*)d_in[6], (const u16*)d_in[7], (const u16*)d_in[8],
                                         (const u16*)d_in[9], (u16*)d_out, (unsigned char*)d_ws);
}
```
